```python
import torch

def _softmax(x, axis=-1):
    return torch.softmax(x, dim=axis)
def _gelu(x):
    return torch.nn.functional.gelu(x, approximate="tanh")
def _silu(x):
    return torch.nn.functional.silu(x)
def _sigmoid(x):
    return torch.sigmoid(x)
def _layernorm(x, axis=-1, eps=1e-5):
    m = x.mean(dim=axis, keepdim=True)
    v = x.var(dim=axis, keepdim=True, unbiased=False)
    return (x - m) / torch.sqrt(v + eps)
def _scatter_add(tgt, idx, src, axis=0):
    return tgt.clone().index_add_(axis, idx, src)
def _segment_sum(src, seg, n):
    out = torch.zeros((n,) + src.shape[1:], dtype=src.dtype)
    return out.index_add_(0, seg, src)
def _pad(x, pw):
    flat = tuple(v for pair in reversed(pw) for v in pair)
    return torch.nn.functional.pad(x, flat)

kernel_name = 'recurrent_scan__ssm_conv_s5__dense_activation_mish__prompt'

def setup_inputs():

    _w = (lambda a: (lambda t: t.to(torch.bfloat16) if t.is_floating_point() else t)(torch.as_tensor(a)))
    def _A_setup():
        B, S, D = 1, 2048, 576
        P = D
        Lambda_re = -0.5 * (lambda _x: torch.exp(torch.as_tensor(_x)))((lambda *s: torch.randn(*s, dtype=torch.bfloat16) * 0.4)(P))
        Lambda_im = (lambda *s: torch.randn(*s, dtype=torch.bfloat16) * 0.8)(P) * 1.0
        B_re = (lambda *s: torch.randn(*s, dtype=torch.bfloat16) * 0.4)(P, D) * (D ** -0.5)
        B_im = (lambda *s: torch.randn(*s, dtype=torch.bfloat16) * 0.8)(P, D) * (D ** -0.5)
        C_re = (lambda *s: torch.randn(*s, dtype=torch.bfloat16) * 0.8)(D, P) * (P ** -0.5)
        C_im = (lambda *s: torch.randn(*s, dtype=torch.bfloat16) * 0.8)(D, P) * (P ** -0.5)
        log_dt = (lambda *s: torch.randn(*s, dtype=torch.bfloat16) * 0.4)(P) * 0.3 - 2.0
        Dskip = (lambda *s: torch.randn(*s, dtype=torch.bfloat16) * 0.2)(D)
        return {
            "x": _w((lambda *s: torch.randn(*s, dtype=torch.bfloat16) * 0.05)(B, S, D)),
            "Lambda_re": _w(Lambda_re),
            "Lambda_im": _w(Lambda_im),
            "B_re": _w(B_re),
            "B_im": _w(B_im),
            "C_re": _w(C_re),
            "C_im": _w(C_im),
            "log_dt": _w(log_dt),
            "Dskip": _w(Dskip),
        }
    _A_in = _A_setup()

    def _B_setup():
        B, S, D, F = 1, 2048, 576, 4608
        return {
            "x": _w((lambda *s: torch.randn(*s, dtype=torch.bfloat16) * 0.05)(B, S, D)),
            "w1": _w((lambda *s: torch.randn(*s, dtype=torch.bfloat16) * 0.8)(D, F) * (D ** -0.5)),
            "b1": _w((lambda *s: torch.randn(*s, dtype=torch.bfloat16) * 0.4)(F)),
            "w2": _w((lambda *s: torch.randn(*s, dtype=torch.bfloat16) * 0.1)(F, D) * (F ** -0.5)),
        }
    _B_in = _B_setup()

    return {
        "A_x": _A_in["x"],
        "A_Lambda_re": _A_in["Lambda_re"],
        "A_Lambda_im": _A_in["Lambda_im"],
        "A_B_re": _A_in["B_re"],
        "A_B_im": _A_in["B_im"],
        "A_C_re": _A_in["C_re"],
        "A_C_im": _A_in["C_im"],
        "A_log_dt": _A_in["log_dt"],
        "A_Dskip": _A_in["Dskip"],
        "B_x": _B_in["x"],
        "B_w1": _B_in["w1"],
        "B_b1": _B_in["b1"],
        "B_w2": _B_in["w2"],
    }

def reference(A_x, A_Lambda_re, A_Lambda_im, A_B_re, A_B_im, A_C_re, A_C_im, A_log_dt, A_Dskip, B_x, B_w1, B_b1, B_w2):
    def _A_ref(x, Lambda_re, Lambda_im, B_re, B_im, C_re, C_im, log_dt, Dskip):
        Bsz, L, H = x.shape
        Lam = Lambda_re + 1j * Lambda_im
        Bmat = B_re + 1j * B_im
        Cmat = C_re + 1j * C_im
        dt = torch.exp(log_dt)
        Lam_bar = torch.exp(dt * Lam)
        B_bar = ((Lam_bar - 1.0) / Lam)[:, None] * Bmat
        Bu = torch.einsum("bld,pd->blp", x + 0j, B_bar)
        h = torch.zeros_like(Bu[:, 0, :])
        hs = []
        for t in range(L):
            h = Lam_bar[None, :] * h + Bu[:, t, :]
            hs.append(h)
        hseq = torch.stack(hs, 1)
        y = 2.0 * torch.einsum("blp,dp->bld", hseq, Cmat).real + x * Dskip[None, None, :]
        return y

    def _B_ref(x, w1, b1, w2):
        h = torch.einsum("bsd,df->bsf", x, w1) + b1
        sp = torch.log1p(torch.exp(-torch.abs(h))) + torch.where(h > 0, h, torch.zeros_like(h))
        act = h * torch.tanh(sp)
        return torch.einsum("bsf,fd->bsd", act, w2)

    x = A_x
    xn = _layernorm(x, axis=-1)
    s = _A_ref(xn, A_Lambda_re, A_Lambda_im, A_B_re, A_B_im, A_C_re, A_C_im, A_log_dt, A_Dskip)
    g = _silu(_B_ref(xn, B_w1, B_b1, B_w2))
    return x + s * g
```

```cpp
#include <hip/hip_runtime.h>
#include <hip/hip_bf16.h>
typedef unsigned short u16;
constexpr int L=2048, D=576, P=576, F=4608;
__device__ __forceinline__ float bf2f(u16 v){ return __uint_as_float(((unsigned)v)<<16); }
__device__ __forceinline__ u16 f2bf(float f){ __hip_bfloat16 b = __float2bfloat16(f); return *reinterpret_cast<u16*>(&b); }
__device__ __forceinline__ float rbf(float f){ return bf2f(f2bf(f)); }

__global__ void ln_k(const u16* x, float* xn){
  int row = blockIdx.x; int lane = threadIdx.x;
  float v[9]; float s=0;
  for(int i=0;i<9;i++){ v[i]=bf2f(x[row*D+lane+64*i]); s+=v[i]; }
  for(int o=32;o;o>>=1) s+=__shfl_xor(s,o);
  float m=s/D; float q=0;
  for(int i=0;i<9;i++){ float d=v[i]-m; q+=d*d; }
  for(int o=32;o;o>>=1) q+=__shfl_xor(q,o);
  float var=rbf(q/D); float mb=rbf(m);
  float sd=rbf(sqrtf(rbf(var+1e-5f)));
  for(int i=0;i<9;i++) xn[row*D+lane+64*i]=rbf(rbf(v[i]-mb)/sd);
}
__global__ void g1_k(const float* xn, const u16* w1, const u16* b1, float* act){
  int f = blockIdx.x*256+threadIdx.x; int l0 = blockIdx.y*8;
  float acc[8]={0};
  for(int k=0;k<D;k++){ float w=bf2f(w1[k*F+f]); for(int i=0;i<8;i++) acc[i]+=xn[(l0+i)*D+k]*w; }
  for(int i=0;i<8;i++){ float h=acc[i]+bf2f(b1[f]); float sp=log1pf(expf(-fabsf(h)))+fmaxf(h,0.f); act[(size_t)(l0+i)*F+f]=rbf(h*tanhf(sp)); }
}
__global__ void bu_k(const float* xn, const u16* Bre, const u16* Bim, float* bur, float* bui){
  int p = blockIdx.x*64+threadIdx.x; int l0 = blockIdx.y*8;
  float ar[8]={0}, ai[8]={0};
  for(int k=0;k<D;k++){ float br=bf2f(Bre[p*D+k]), bi=bf2f(Bim[p*D+k]); for(int i=0;i<8;i++){ float xv=xn[(l0+i)*D+k]; ar[i]+=xv*br; ai[i]+=xv*bi; } }
  for(int i=0;i<8;i++){ bur[(l0+i)*P+p]=ar[i]; bui[(l0+i)*P+p]=ai[i]; }
}
__global__ void scan_k(const u16* Lre, const u16* Lim, const u16* ldt, float* bur, float* bui){
  int p = blockIdx.x*64+threadIdx.x;
  float lr=bf2f(Lre[p]), li=bf2f(Lim[p]); float dt=rbf(expf(bf2f(ldt[p])));
  float er=expf(dt*lr); float lbr=er*cosf(dt*li), lbi=er*sinf(dt*li);
  float nr=lbr-1.f, ni=lbi; float den=lr*lr+li*li;
  float cr=(nr*lr+ni*li)/den, ci=(ni*lr-nr*li)/den;
  float hr=0,hi=0;
  for(int t=0;t<L;t++){ float ur=bur[t*P+p], ui=bui[t*P+p];
    float br=cr*ur-ci*ui, bi=cr*ui+ci*ur;
    float nhr=lbr*hr-lbi*hi+br, nhi=lbr*hi+lbi*hr+bi; hr=nhr; hi=nhi; bur[t*P+p]=hr; bui[t*P+p]=hi; }
}
__global__ void out_k(const u16* x, const float* xn, const float* act, const u16* w2, const float* hr, const float* hi, const u16* Cre, const u16* Cim, const u16* Dsk, float* out){
  int d = blockIdx.x*64+threadIdx.x; int l0 = blockIdx.y*4;
  float acc[4]={0}, ys[4]={0};
  for(int k=0;k<F;k++){ float w=bf2f(w2[k*D+d]); for(int i=0;i<4;i++) acc[i]+=act[(size_t)(l0+i)*F+k]*w; }
  for(int p=0;p<P;p++){ float cr=bf2f(Cre[d*P+p]), ci=bf2f(Cim[d*P+p]); for(int i=0;i<4;i++) ys[i]+=hr[(l0+i)*P+p]*cr-hi[(l0+i)*P+p]*ci; }
  for(int i=0;i<4;i++){ float f=rbf(acc[i]); float g=rbf(f/(1.f+expf(-f))); float s=2.f*ys[i]+rbf(xn[(l0+i)*D+d]*bf2f(Dsk[d])); out[(l0+i)*D+d]=bf2f(x[(l0+i)*D+d])+s*g; }
}
extern "C" void kernel_launch(void* const* d_in, const int* in_sizes, int n_in, void* d_out, int out_size, void* d_ws, size_t ws_size, hipStream_t stream){
  const u16* x=(const u16*)d_in[0]; const u16* Lre=(const u16*)d_in[1]; const u16* Lim=(const u16*)d_in[2];
  const u16* Bre=(const u16*)d_in[3]; const u16* Bim=(const u16*)d_in[4]; const u16* Cre=(const u16*)d_in[5]; const u16* Cim=(const u16*)d_in[6];
  const u16* ldt=(const u16*)d_in[7]; const u16* Dsk=(const u16*)d_in[8];
  const u16* w1=(const u16*)d_in[10]; const u16* b1=(const u16*)d_in[11]; const u16* w2=(const u16*)d_in[12];
  float* ws=(float*)d_ws; float* xn=ws; float* bur=xn+L*D; float* bui=bur+L*P; float* act=bui+L*P;
  ln_k<<<L,64,0,stream>>>(x,xn);
  g1_k<<<dim3(F/256,L/8),256,0,stream>>>(xn,w1,b1,act);
  bu_k<<<dim3(P/64,L/8),64,0,stream>>>(xn,Bre,Bim,bur,bui);
  scan_k<<<P/64,64,0,stream>>>(Lre,Lim,ldt,bur,bui);
  out_k<<<dim3(D/64,L/4),64,0,stream>>>(x,xn,act,w2,bur,bui,Cre,Cim,Dsk,(float*)d_out);
}
```

```cpp
#include <hip/hip_runtime.h>
typedef short bf16x8 __attribute__((ext_vector_type(8)));
typedef short s16x4 __attribute__((ext_vector_type(4)));
typedef float f32x16 __attribute__((ext_vector_type(16)));
typedef float f32x4 __attribute__((ext_vector_type(4)));
typedef float f32x2 __attribute__((ext_vector_type(2)));
typedef unsigned u32x4 __attribute__((ext_vector_type(4)));
typedef unsigned u32x2 __attribute__((ext_vector_type(2)));
typedef __bf16 bf16x2 __attribute__((ext_vector_type(2)));
typedef unsigned long long u64;
#define LAS __attribute__((address_space(3)))
#define GAS __attribute__((address_space(1)))
typedef LAS char* lptr;
#define DI __device__ __forceinline__
#define SBAR() __builtin_amdgcn_sched_barrier(0)
#define PIN(x) asm volatile("" : "+v"(x))
#define MFMA(a, b, c) __builtin_amdgcn_mfma_f32_32x32x16_bf16(a, b, c, 0, 0, 0)
#define FMA(a, b, c) __builtin_fmaf(a, b, c)

constexpr int SLOT = 24576, AUX = 6 * SLOT, LDS_BYTES = AUX + 16384;
constexpr size_t PART_SLICE = 2048ull * 576 * 2;
constexpr size_t PRM_SLICE = 9216 + 8 * 3072;
constexpr size_t WS_PART = 0, WS_STAT = 7 * PART_SLICE, WS_COR = WS_STAT + 2048 * 8, WS_PRM = WS_COR + 15 * 6144, WS_GRAN = WS_PRM + 3 * PRM_SLICE, WS_ACK = WS_GRAN + 9 * 16 * 1024, WS_DONE = WS_ACK + 1024, WS_EPOCH = WS_DONE + 16 * 256, WS_PS = WS_EPOCH + 4096, WS_H = WS_PS + 64 * 8192;
constexpr unsigned SPIN_MAX = 1u << 14;


#define glds16(sbase, voff, lds, imm) asm volatile("s_add_u32 m0, %2, %3\n\ts_nop 0\n\tglobal_load_lds_dwordx4 %0, %1" :: "v"(voff), "s"(sbase), "s"(lds), "i"(imm) : "memory", "scc")
#define ALD4(dst, voff, sbase) asm volatile("global_load_dwordx4 %0, %1, %2 sc1" : "=v"(dst) : "v"(voff), "s"(sbase) : "memory")
#define ALD4B(dst, voff, sbase) ALD4(dst, voff, sbase)
#define PINH(a, b, c, d, e, f) asm volatile("; VW %0 %1 %2 %3 %4 %5" : "+v"(a), "+v"(b), "+v"(c), "+v"(d), "+v"(e), "+v"(f))
#define ALD2(dst, voff, sbase) asm volatile("global_load_dwordx2 %0, %1, %2 sc1" : "=v"(dst) : "v"(voff), "s"(sbase) : "memory")
#define ALD1(dst, voff, sbase) asm volatile("global_load_dword %0, %1, %2" : "=v"(dst) : "v"(voff), "s"(sbase) : "memory")
#define VWAIT1(n, a) asm volatile("s_waitcnt vmcnt(" #n ") ; VW %0" : "+v"(a) :: "memory")
#define VWAIT2(n, a, b) asm volatile("s_waitcnt vmcnt(" #n ") ; VW %0 %1" : "+v"(a), "+v"(b) :: "memory")
#define VWAIT3(n, a, b, c) asm volatile("s_waitcnt vmcnt(" #n ") ; VW %0 %1 %2" : "+v"(a), "+v"(b), "+v"(c) :: "memory")
#define LANE2(n) int n; asm volatile("v_mbcnt_lo_u32_b32 %0, -1, 0\n\tv_mbcnt_hi_u32_b32 %0, -1, %0" : "=v"(n))
template <class R> DI void st16(u32x4 v, R rs, unsigned voff, unsigned soff, int aux) { __builtin_amdgcn_raw_buffer_store_b128(v, rs, voff, soff, 16); asm volatile("s_nop 1" :: "v"(v) : "memory"); }
DI bf16x8 ldb128(lptr p) { return *(const LAS bf16x8*)p; }
DI f32x4 ldf4(lptr p) { return *(const LAS f32x4*)p; }
DI f32x2 ldf2(lptr p) { return *(const LAS f32x2*)p; }
DI void stb128(lptr p, bf16x8 v) { *(LAS bf16x8*)p = v; }
DI void stu4(lptr p, u32x4 v) { *(LAS u32x4*)p = v; }
DI void stf2(lptr p, float a, float b) { *(LAS f32x2*)p = (f32x2){a, b}; }
DI void stf32(lptr p, float v) { *(LAS float*)p = v; }
DI bf16x8 trfrag(lptr p, int hioff) {
    const s16x4 lo = __builtin_amdgcn_ds_read_tr16_b64_v4i16((LAS s16x4*)p), hi = __builtin_amdgcn_ds_read_tr16_b64_v4i16((LAS s16x4*)(p + hioff));
    return __builtin_shufflevector(lo, hi, 0, 1, 2, 3, 4, 5, 6, 7); }
DI bf16x8 trfrag2(lptr p, lptr q) {
    const s16x4 lo = __builtin_amdgcn_ds_read_tr16_b64_v4i16((LAS s16x4*)p), hi = __builtin_amdgcn_ds_read_tr16_b64_v4i16((LAS s16x4*)q);
    return __builtin_shufflevector(lo, hi, 0, 1, 2, 3, 4, 5, 6, 7); }
DI unsigned cvtpk(float lo, float hi) { const bf16x2 v = {(__bf16)lo, (__bf16)hi}; return __builtin_bit_cast(unsigned, v); }
DI float rbf(float f) { return (float)(__bf16)f; }
DI float bflo(unsigned u) { return __uint_as_float(u << 16); }
DI float bfhi(unsigned u) { return __uint_as_float(u & 0xffff0000u); }
DI float dot2(unsigned a, unsigned b, float c) { return __builtin_amdgcn_fdot2_f32_bf16(__builtin_bit_cast(bf16x2, a), __builtin_bit_cast(bf16x2, b), c, false); }
DI f32x2 cmul(f32x2 a, f32x2 b) { return (f32x2){a.x * b.x - a.y * b.y, a.x * b.y + a.y * b.x}; }
DI float sum_halves(float v) { auto r = __builtin_amdgcn_permlane32_swap(__float_as_uint(v), __float_as_uint(v), false, false); return __uint_as_float(r[0]) + __uint_as_float(r[1]); }
DI unsigned next_tag(unsigned e) { return ((e | 0x40004000u) + 1u) & ~0x40004000u; }
DI u32x4 mkcorr(float s, unsigned bias, unsigned tag) {
    const unsigned h0 = __float_as_uint(s) & 0xffff0000u; const float r1 = s - __uint_as_float(h0);
    const unsigned h1 = __float_as_uint(r1) & 0xffff0000u; const float r2 = r1 - __uint_as_float(h1);
    return (u32x4){(h0 >> 16) | h1, tag, (__float_as_uint(r2) >> 16) | (bias << 16), tag}; }
DI u32x4 repoll4(const char* base, unsigned voff, unsigned tag, bool need) {
    asm volatile("s_waitcnt vmcnt(0)" ::: "memory"); auto rs = __builtin_amdgcn_make_buffer_rsrc((void*)base, 0, 0x100000, 0x00020000); u32x4 g;
    for (unsigned s = 0; s < SPIN_MAX; ++s) { g = __builtin_amdgcn_raw_buffer_load_b128(rs, voff, 0, 16); if (__all(!need || (g[1] == tag && g[3] == tag))) break; __builtin_amdgcn_s_sleep(8); asm volatile("" ::: "memory"); }
    return g; }
struct Red { u32x4 v[7]; unsigned st; u32x2 xv[2], dv[2]; f32x16 s; };
template <int I0, int I1> DI void red_load(Red& r, int rt, int ct, int g, int half, int lane, char* ws) {
    auto rs = __builtin_amdgcn_make_buffer_rsrc(ws, 0, (unsigned)WS_COR, 0x00020000);
#pragma unroll
    for (int i = I0; i < I1; ++i) r.v[i] = __builtin_amdgcn_raw_buffer_load_b128(rs, (2 * g + half) * 1024 + lane * 16, ((rt * 18 + ct) * 7 + (i < 4 ? (g + i) & 3 : i)) * 8192, 16); }
DI bf16x8 red_sel(int lane, int upper) {
    const int m = lane & 31, j = (m & 3) + 4 * ((m >> 3) & 1); const bool on = (m >> 4) == upper && ((m >> 2) & 1) == (lane >> 5); const unsigned w = on ? ((j & 1) ? 0x3f800000u : 0x3f80u) : 0u;
    return __builtin_bit_cast(bf16x8, (u32x4){(j >> 1) == 0 ? w : 0u, (j >> 1) == 1 ? w : 0u, (j >> 1) == 2 ? w : 0u, (j >> 1) == 3 ? w : 0u}); }
template <int I0, int I1> DI void red_sum(Red& r, bf16x8 sel) {
    if (I0 == 0) r.s = (f32x16){0.f, 0.f, 0.f, 0.f, 0.f, 0.f, 0.f, 0.f, 0.f, 0.f, 0.f, 0.f, 0.f, 0.f, 0.f, 0.f};
#pragma unroll
    for (int i = I0; i < I1; ++i) r.s = MFMA(sel, __builtin_bit_cast(bf16x8, r.v[i]), r.s); }
DI void red_gate(Red& r) {
    const float mb = bflo(r.st), rs = __builtin_amdgcn_rcpf(bfhi(r.st));
#pragma unroll
    for (int i = 0; i < 8; ++i) { const unsigned xu = r.xv[i >> 2][(i >> 1) & 1], du = r.dv[i >> 2][(i >> 1) & 1]; const float xx = (i & 1) ? bfhi(xu) : bflo(xu), dd = (i & 1) ? bfhi(du) : bflo(du);
        const float f = r.s[i]; r.s[i] = f * 0.693147180559945309f * __builtin_amdgcn_rcpf(1.f + __builtin_amdgcn_exp2f(-f));
        r.s[8 + i] = (xx - mb) * rs * dd; }
    PIN(r.s); }
DI void red_finish(const Red& r, int half, int lane, lptr img) {
#pragma unroll
    for (int g = 0; g < 2; ++g) { f32x4 o;
#pragma unroll
        for (int j = 0; j < 4; ++j) { const unsigned xu = r.xv[g][j >> 1]; o[j] = FMA(r.s[8 + 4 * g + j], r.s[4 * g + j], (j & 1) ? bfhi(xu) : bflo(xu)); }
        *(LAS f32x4*)(img + (lane & 31) * 128 + (((4 * half + 2 * g + (lane >> 5)) ^ (lane & 7)) << 4)) = o; } }
DI void wait_prog(const u64* p, unsigned need, unsigned tag) {
    for (unsigned s = 0; s < SPIN_MAX; ++s) { const u64 g = __hip_atomic_load(p, __ATOMIC_RELAXED, __HIP_MEMORY_SCOPE_AGENT);
        if (__all((unsigned)(g >> 32) == tag && (unsigned)g >= need)) break; __builtin_amdgcn_s_sleep(2); } }
#define BARRIER() asm volatile("s_waitcnt lgkmcnt(0)\n\ts_barrier" ::: "memory")

__global__ __launch_bounds__(512, 2) void k1(const char* __restrict__ x, const char* __restrict__ lre, const char* __restrict__ lim, const char* __restrict__ bre, const char* __restrict__ bim,
        const char* __restrict__ cre, const char* __restrict__ cim, const char* __restrict__ ldt, const char* __restrict__ w1, const char* __restrict__ b1, const char* __restrict__ w2, const char* __restrict__ dsk, char* __restrict__ ws, float* __restrict__ out) {
    extern __shared__ __attribute__((aligned(16))) char shm[];
    const int wave = __builtin_amdgcn_readfirstlane(threadIdx.x >> 6);
    const int xi = blockIdx.x & 7, wi = blockIdx.x >> 3;
    const lptr L = (lptr)shm;
    int f_rt, f_me, f_ct = 0, f_jj = 0; unsigned f_tag;
    const bool grp_ssm = wi < 8;
    const int nh = 2, hi_ = 2 * (xi & 1) + wi;
    if (wi < nh) {
        LANE2(lane); const int tid = wave * 64 + lane, sl = hi_ < 3 ? 3 * (xi >> 1) + hi_ : 12 + (xi >> 1);
        unsigned tag;
        if (sl == 15) {
            tag = next_tag(__builtin_amdgcn_readfirstlane(__hip_atomic_load((const unsigned*)(ws + WS_EPOCH), __ATOMIC_RELAXED, __HIP_MEMORY_SCOPE_AGENT)));
#pragma unroll 1
            for (int g = wave; g < 9; g += 8) { const int ch = g * 64 + lane, s3 = g / 3, q = g - 3 * s3; const unsigned vo = (q * 64 + lane) * 16;
                const float lr = bflo(*(const unsigned short*)(lre + ch * 2)), lm = bflo(*(const unsigned short*)(lim + ch * 2)), dt = rbf(expf(bflo(*(const unsigned short*)(ldt + ch * 2)))), ea = expf(dt * lr);
                float sn, cn; sincosf(dt * lm, &sn, &cn);
                const float br = ea * cn, bi = ea * sn, nr = br - 1.f, den = 2.f / (lr * lr + lm * lm);
                auto prs = __builtin_amdgcn_make_buffer_rsrc(ws + WS_PRM + s3 * PRM_SLICE, 0, (unsigned)PRM_SLICE, 0x00020000);
                f32x2 l16 = {br, bi}, w = {1.f, 0.f}; l16 = cmul(l16, l16); l16 = cmul(l16, l16); l16 = cmul(l16, l16); l16 = cmul(l16, l16);
#pragma unroll
                for (int s = 0; s < 8; ++s) { st16(__builtin_bit_cast(u32x4, (f32x4){l16.x, l16.y, w.x, w.y}), prs, vo, 9216 + s * 3072, 16); w = cmul(w, l16); }
                const f32x2 w2_ = cmul(w, w);
                st16(__builtin_bit_cast(u32x4, (f32x4){w.x, w.y, w2_.x, w2_.y}), prs, vo, 6144, 16);
                st16((u32x4){0u, tag, 0u, tag}, __builtin_amdgcn_make_buffer_rsrc(ws + WS_GRAN, 0, 9 * 16384, 0x00020000), lane * 16, g * 16384 + 15 * 1024, 16);
                asm volatile("s_waitcnt vmcnt(0)" ::: "memory");
                st16((u32x4){__float_as_uint(br), tag, __float_as_uint(bi), tag}, prs, vo, 0, 16);
                st16((u32x4){__float_as_uint((nr * lr + bi * lm) * den), tag, __float_as_uint((bi * lr - nr * lm) * den), tag}, prs, vo, 3072, 16); }
        } else {
        const int sc = sl; const bool mlp = sl < 12;
        const unsigned ldsR = (unsigned)(size_t)L + wave * 1024;
        const unsigned voffR = (8 * wave + (lane >> 3)) * 1152 + (((lane & 7) ^ ((4 * wave + (lane >> 4)) & 7)) << 4);
        const unsigned voffW = mlp ? (8 * wave + (lane >> 3)) * 9216 + (((lane & 7) ^ (((lane >> 4) & 1) << 2)) << 4) : voffR;
        const char* wp0 = mlp ? w1 + sc * 768 : bre + (size_t)(sc - 12) * (192 * 1152); const char* wp1 = mlp ? wp0 + 128 : wp0 + (bim - bre);
        const unsigned wsi = mlp ? 64 * 9216 : 128, wsp = mlp ? 256 : 64 * 1152;
#define HDMA(c, kt, so) do { const char* b_ = (((c) & 1) ? wp1 : wp0) + ((c) >> 1) * wsp + (kt) * 3 * wsi; const unsigned d_ = ldsR + (so); \
        glds16(b_, voffW, d_, 0); glds16(b_ + wsi, voffW, d_, 8192); glds16(b_ + 2 * wsi, voffW, d_, 16384); } while (0)
        const int cgh = wave & 1, h = lane >> 5, l31 = lane & 31; unsigned ep, bias;
        ALD1(ep, 0u, ws + WS_EPOCH);
        asm volatile("global_load_ushort %0, %1, %2" : "=v"(bias) : "v"(((mlp ? sl : 0) * 384 + (wave < 6 ? wave : 0) * 64 + lane) * 2), "s"(b1) : "memory");
        HDMA((mlp ? 1 : 1), 0, 0); HDMA((mlp ? 1 : 1), 1, SLOT); HDMA((mlp ? 1 : 1), 2, 2 * SLOT);
        HDMA((mlp ? 1 : 1) + 1, 0, 3 * SLOT); HDMA((mlp ? 1 : 1) + 1, 1, 4 * SLOT);
        VWAIT2(15, ep, bias);
        tag = next_tag(__builtin_amdgcn_readfirstlane(ep));
        bias = mlp ? bias : 0u;
        auto crs = __builtin_amdgcn_make_buffer_rsrc(ws + WS_COR + sc * 6144, 0, 6144, 0x00020000);
        const int q4 = (lane & 15) >> 2, wr = 32 * cgh + l31, kq = wave >> 1;
        const unsigned atr = (8 * h + q4) * 128 + (((4 * cgh + 2 * ((lane >> 4) & 1) + ((lane & 3) >> 1)) ^ ((q4 >> 1) << 2)) << 4) + 8 * (lane & 1) + kq * 2048;
        const unsigned arb = (wr * 128 + ((h ^ ((wr >> 1) & 7)) << 4)) ^ (kq << 5);
        const bf16x8 ones = {0x3f80, 0x3f80, 0x3f80, 0x3f80, 0x3f80, 0x3f80, 0x3f80, 0x3f80};
#pragma unroll 1
        for (int c = (mlp ? 1 : 1); c < 7; ++c) {
            const int so = ((c - (mlp ? 1 : 1)) & 1) * 3 * SLOT, sn = 3 * SLOT - so;
            f32x16 a = {0.f, 0.f, 0.f, 0.f, 0.f, 0.f, 0.f, 0.f, 0.f, 0.f, 0.f, 0.f, 0.f, 0.f, 0.f, 0.f};
#pragma unroll
            for (int kt = 0; kt < 3; ++kt) {
                if (kt == 0) { asm volatile("s_waitcnt lgkmcnt(0)\n\ts_barrier" ::: "memory");
                    if (c > (mlp ? 1 : 1) && wave == c - 1) { const f32x4 v = ldf4(L + AUX + lane * 16); st16(mkcorr((v[0] + v[1]) + (v[2] + v[3]), bias, tag), crs, lane * 16, (c - 1) * 1024, 16); }
                    if (c == 6) break; }
                asm volatile("s_waitcnt vmcnt(12) lgkmcnt(0)\n\ts_barrier" ::: "memory");
                const lptr p = L + so + kt * SLOT; bf16x8 f0, f1, f2;
                if (mlp) { f0 = trfrag(p + atr, 512); f1 = trfrag(p + atr + 8192, 512); f2 = trfrag(p + atr + 16384, 512); }
                else { f0 = ldb128(p + arb); f1 = ldb128(p + arb + 8192); f2 = ldb128(p + arb + 16384); }
                { const int cn = kt == 0 ? c + 1 : c + 2, cc = cn < 6 ? cn : 5; if (kt == 0) HDMA(cc, 2, sn + 2 * SLOT); else HDMA(cc, kt - 1, so + (kt - 1) * SLOT); }
                a = MFMA(ones, f0, a); a = MFMA(ones, f1, a); a = MFMA(ones, f2, a); }
            stf32(L + AUX + wr * 16 + kq * 4, a[0]);
        }
        asm volatile("s_waitcnt vmcnt(0)" ::: "memory");
        }
        BARRIER();
        if (tid == 0) __hip_atomic_store((unsigned*)(ws + WS_ACK) + blockIdx.x, tag, __ATOMIC_RELAXED, __HIP_MEMORY_SCOPE_AGENT);
        if (sl == 15 && wave == 1) {
            auto ars = __builtin_amdgcn_make_buffer_rsrc(ws + WS_ACK, 0, 1024, 0x00020000);
            for (unsigned s = 0; s < SPIN_MAX; ++s) { const u32x4 g = __builtin_amdgcn_raw_buffer_load_b128(ars, lane * 16, 0, 16);
                if (__all(g[0] == tag && g[1] == tag && g[2] == tag && g[3] == tag)) break; __builtin_amdgcn_s_sleep(16); asm volatile("" ::: "memory"); }
            if (lane == 0) __hip_atomic_store((unsigned*)(ws + WS_EPOCH), tag, __ATOMIC_RELAXED, __HIP_MEMORY_SCOPE_AGENT);
        }
        f_rt = 4 * (xi >> 1) + hi_; f_me = 15; f_tag = tag;
    } else {
    if (wave < 3) {
        u64 pc_; int e_; asm volatile("s_getpc_b64 %0\nk1_here:\n\ts_mov_b32 %1, k1_end-k1_here" : "=s"(pc_), "=s"(e_));
        e_ -= 16; LANE2(l_); int o_ = ((wi - nh) * 3 + wave) * 1024 + l_ * 16; o_ = o_ < e_ ? o_ : 0;
        glds16((const char*)(pc_ & ~15ull), o_, (unsigned)(size_t)L + wave * 1024, AUX + 4096); }
    const int fl = (wi - 8) / 3, mem = wi - 8 - 3 * fl, fg = xi * 8 + fl, si = 6 * (xi >> 1) + wi - 2;
    const int slice = grp_ssm ? 12 + (si >> 3) : 3 * (fg >> 4) + mem, rt = grp_ssm ? 8 * (xi & 1) + (si & 7) : fg & 15;
    const unsigned ldsR = (unsigned)(size_t)L + wave * 1024;
    unsigned voffR; { LANE2(l_); voffR = (8 * wave + (l_ >> 3)) * 1152 + (((l_ & 7) ^ ((4 * wave + (l_ >> 4)) & 7)) << 4); }
    const char* gx = x + (size_t)rt * (128 * 1152);
#define GL(base, off, voff, dst) glds16((base) + (unsigned)(off), voff, ldsR, dst)
    const int rk3 = (slice + 1 * rt) % 3;   const unsigned kx0 = rk3 * 384, kx1 = (rk3 == 2 ? 0 : rk3 + 1) * 384, kx2 = (rk3 == 0 ? 2 : rk3 - 1) * 384;
    GL(gx, kx0, voffR, 4 * SLOT); GL(gx, kx0 + 64 * 1152, voffR, 5 * SLOT);
    const int rg = wave & 3, cg = wave >> 2;
#define ROLE_IDS() LANE2(lane); const int tid = wave * 64 + lane, h = lane >> 5, l31 = lane & 31; unsigned voffC = (cg * 32 + l31) * 16; PIN(voffC); PIN(ones8)
    const int rj9 = rt % 9;
#define ROT9(j) ((j) + rj9 >= 9 ? (j) + rj9 - 9 : (j) + rj9)
    auto prs = __builtin_amdgcn_make_buffer_rsrc(ws + WS_PART + (size_t)((rt * 18 + cg + (grp_ssm ? 0 : 6 * mem)) * 7 + (grp_ssm ? slice - 8 : fg >> 4)) * 8192 + rg * 2048, 0, !grp_ssm && rg == (fg >> 4) ? 0 : 8 * 14 * 8192 + 2048, 0x00020000);
#define DEF_PRS() LANE2(ln_p); const int voffS = ln_p * 16
#define stpart(v, off) st16(v, prs, voffS, off, 16)
    const f32x16 zero16 = {0.f, 0.f, 0.f, 0.f, 0.f, 0.f, 0.f, 0.f, 0.f, 0.f, 0.f, 0.f, 0.f, 0.f, 0.f, 0.f};
    bf16x8 xf[36], wf0, wf1, wf2, wf3, wf4, wf5, wf6, wf7, wf8, wf9, wf10, wf11, lnf; f32x16 acc0, acc1; u32x4 pk0, pk1, cor, cor2; u32x2 cv, sg0, sg1; unsigned ep, tag; float rr;
    bf16x8 cf0, cf1, ones8 = {0x3f80, 0x3f80, 0x3f80, 0x3f80, 0x3f80, 0x3f80, 0x3f80, 0x3f80}; f32x16 csa; f32x4 csv;
    const char* gco = ws + WS_COR + slice * 6144;
#define GOT_EPOCH() do { tag = next_tag(__builtin_amdgcn_readfirstlane(ep)); if (tid == 0) __hip_atomic_store((unsigned*)(ws + WS_ACK) + blockIdx.x, tag, __ATOMIC_RELAXED, __HIP_MEMORY_SCOPE_AGENT); } while (0)
#define CHK_COR(v, c) do { if (__builtin_expect(__any(v[1] != tag || v[3] != tag), 0)) { v = repoll4(gco + (c) * 1024, voffC, tag, true); } } while (0)
    float sa0 = 0.f, sa1 = 0.f, sa2 = 0.f, sa3 = 0.f, sto; unsigned stw;
#define STAT_PART(k0, k1) do { if (cg) { _Pragma("unroll") for (int k = k0; k < k1; ++k) { const u32x4 u = __builtin_bit_cast(u32x4, xf[k]); \
            asm volatile("v_dot2c_f32_bf16 %0, %4, %4\n\tv_dot2c_f32_bf16 %1, %5, %5\n\tv_dot2c_f32_bf16 %2, %6, %6\n\tv_dot2c_f32_bf16 %3, %7, %7" : "+v"(sa0), "+v"(sa1), "+v"(sa2), "+v"(sa3) : "v"(u[0]), "v"(u[1]), "v"(u[2]), "v"(u[3])); } } \
        else { _Pragma("unroll") for (int k = k0; k < k1; ++k) { const u32x4 u = __builtin_bit_cast(u32x4, xf[k]); \
            asm volatile("v_dot2c_f32_bf16 %0, %8, %4\n\tv_dot2c_f32_bf16 %1, %8, %5\n\tv_dot2c_f32_bf16 %2, %8, %6\n\tv_dot2c_f32_bf16 %3, %8, %7" : "+v"(sa0), "+v"(sa1), "+v"(sa2), "+v"(sa3) : "v"(u[0]), "v"(u[1]), "v"(u[2]), "v"(u[3]), "s"(0x3f803f80u)); } } \
        asm volatile("s_nop 3" : "+v"(sa0), "+v"(sa1), "+v"(sa2), "+v"(sa3)); } while (0)
    const char* gps = ws + WS_PS + (grp_ssm ? rt : fg) * 8192; float tx, tq; unsigned lw0;
#define STAT_PUT() do { sa0 = sum_halves((sa0 + sa1) + (sa2 + sa3)); \
        __builtin_amdgcn_raw_buffer_store_b64((u32x2){__float_as_uint(sa0), tag}, __builtin_amdgcn_make_buffer_rsrc((void*)gps, 0, 8192, 0x00020000), h ? 0x10000 : voffT, (cg * 3 + mem) * 1024, 16); } while (0)
#define RE2(v, o) do { const u64* p_ = (const u64*)(gps + (o) + voffT); wait_prog(p_, 0u, tag); v[0] = (unsigned)__hip_atomic_load(p_, __ATOMIC_RELAXED, __HIP_MEMORY_SCOPE_AGENT); PIN(v); } while (0)
#define CHK_STAT() do { if (__builtin_expect(__any(sg0[1] != tag || sg1[1] != tag || cv[1] != tag), 0)) { asm volatile("s_waitcnt vmcnt(0)" ::: "memory"); RE2(sg0, 0); RE2(sg1, 1024); RE2(cv, 2048); } } while (0)
#define FS1() do { tx = __uint_as_float(sg0[0]) + __uint_as_float(sg1[0]); PIN(tx); } while (0)
#define FS2() do { tx += __uint_as_float(cv[0]); PIN(tx); } while (0)
#define FS3() do { auto r_ = __builtin_amdgcn_permlane32_swap(__float_as_uint(tx), __float_as_uint(tx), false, false); tx = __uint_as_float(r_[0]); tq = __uint_as_float(r_[1]); PIN(tx); PIN(tq); } while (0)
#define FS4() do { sto = tx * (1.f / 576.f); PIN(sto); tq *= (1.f / 576.f); PIN(tq); } while (0)
#define FS5() do { tq = FMA(-sto, sto, tq); PIN(tq); } while (0)
#define FS6() do { stw = cvtpk(sto, __builtin_fmaxf(tq, 0.f)); PIN(stw); } while (0)
#define FS7() do { tq = bfhi(stw) + 1e-5f; PIN(tq); } while (0)
#define FS8() do { tq = rbf(tq); PIN(tq); } while (0)
#define FS9() do { tq = __builtin_amdgcn_sqrtf(tq); PIN(tq); lw0 = (stw & 0xffffu) ^ 0x8000u; PIN(lw0); } while (0)
#define FS10() do { stw = cvtpk(sto, tq); PIN(stw); lw0 = h ? 0u : lw0 | (lw0 << 16); PIN(lw0); } while (0)
#define FS11A() { rr = __builtin_amdgcn_rcpf(bfhi(stw)); const u32x4 w_ = {lw0, 0u, h ? 0u : stw ^ 0x8000u, 0u}; lnf = __builtin_bit_cast(bf16x8, w_); PIN(lnf); }
#define PROG(n) do { if (wave == 0) { LANE2(l_); if (l_ == 0) __hip_atomic_store((u64*)(ws + WS_DONE) + rt * 32 + slice, ((u64)tag << 32) | (n), __ATOMIC_RELAXED, __HIP_MEMORY_SCOPE_AGENT); } } while (0)
#define CORF2 __builtin_bit_cast(bf16x8, cor2)
#define CORF __builtin_bit_cast(bf16x8, cor)

    if (slice < 12) {
        ROLE_IDS(); const int blk = (lane >> 4) & 1, q4 = (lane & 15) >> 2, p2 = lane & 3;
        const unsigned voffK1 = (8 * wave + (lane >> 3)) * 9216 + (((lane & 7) ^ (((lane >> 4) & 1) << 2)) << 4);
#define DEF_K2() LANE2(ln_k); const unsigned voffK2 = (8 * wave + (ln_k >> 3)) * 1152 + (((ln_k & 7) ^ (((ln_k >> 4) & 1) << 2)) << 4)
        const unsigned kw0 = kx0 * 4608, kw1 = kx1 * 4608, kw2 = kx2 * 4608;
        const char* gw1 = w1 + slice * 768; const char* gw2 = w2 + (size_t)(slice - mem) * (384 * 1152) + mem * 384;
        const int rj3 = rt % 3;
#define ROT3(j) ((j) + rj3 >= 3 ? (j) + rj3 - 3 : (j) + rj3)
        const int grp = fg >> 4, fjj = wave < 6 ? wave >> 1 : 0, fct = 6 * mem + 2 * ROT3(fjj) + (wave & 1);
#define SRC(p) ((p) + mem >= 3 ? (p) + mem - 3 : (p) + mem)
        const char* ghb = ws + WS_H + (size_t)(fg * 3) * 98304 + wave * 12288; const char* gh1 = ghb + SRC(1) * 98304; const char* gh2 = ghb + SRC(2) * 98304;
        auto hrs = __builtin_amdgcn_make_buffer_rsrc((void*)(ghb + mem * 98304), 0, 12288, 0x00020000);
#define PF2(i) glds16(w2 + (size_t)slice * (384 * 1152) + (rt * 4 + (i) < 54 ? rt * 4 + (i) : 0) * 8192 + wave * 1024, voffS, ldsR, AUX + 8192)
#define PFC() do { const int n_ = fg * 3 + mem; glds16((n_ < 81 ? cre + n_ * 8192 : cim + (n_ < 162 ? n_ - 81 : 0) * 8192) + wave * 1024, voffS, ldsR, AUX + 8192); } while (0)
#define XST(i) glds16((i) && cg ? dsk + mem * 384 : gx + (grp * 32 + 8 * rg) * 1152 + mem * 384 + ((i) ? 256 : cg * 128), (i) && cg ? (voffS < 384 ? voffS : 0) : (voffS >> 7) * 1152 + (voffS & 127), ldsR, 4 * SLOT + 32768 + (i) * 8192)
#define PF1(i) do { const int n_ = (i) * 8 + wave; glds16(gw1 + 256 + (size_t)(rt * 36 + (n_ < 18 ? 2 * n_ : 0)) * 9216, (voffS >> 9) * 9216 + (voffS & 511), ldsR, AUX + 8192); } while (0)
#define PFB() do { const int n_ = fg * 3 + mem, c_ = n_ < 108 ? n_ : 0, a_ = c_ / 18; glds16(((a_ & 1) ? bim : bre) + ((a_ >> 1) * 192 + 64) * 1152 + (c_ - 18 * a_) * 8192 + wave * 1024, voffS, ldsR, AUX + 8192); } while (0)
#define sth(v, off) st16(__builtin_bit_cast(u32x4, v), hrs, voffS, off, 16)
        const char* gdn = ws + WS_DONE + rt * 256 + (slice - mem) * 8;
#define voffG ((voffS & 16) ? SRC(1) * 8 : SRC(2) * 8)
#define CHK_H(need) do { if (__builtin_expect(__any(cv[1] != tag || cv[0] < (need)), 0)) { asm volatile("s_waitcnt vmcnt(0)" ::: "memory"); wait_prog((const u64*)(gdn + voffG), need, tag); } asm volatile("" ::: "memory"); } while (0)
        const int xr = 32 * (rg & 1) + l31; const unsigned rxo = (rg >> 1) * SLOT + xr * 128 + ((h ^ ((xr >> 1) & 7)) << 4);
        lptr rx0 = L + rxo, rx1 = L + (rxo ^ 32u), rx2 = L + (rxo ^ 64u), rx3 = L + (rxo ^ 96u);
        lptr trA = L + (8 * h + q4) * 128 + (((4 * cg + 2 * blk + (p2 >> 1)) ^ ((q4 >> 1) << 2)) << 4) + 8 * (p2 & 1);
        lptr trCS = trA + rg * 2048; unsigned bv0, bv1;
#define BIAS_LOADS() asm volatile("global_load_ushort %0, %2, %3\n\tglobal_load_ushort %1, %2, %3 offset:128" : "=&v"(bv0), "=&v"(bv1) : "v"(voffC >> 3), "s"(b1 + slice * 768) : "memory")
#define DEF_TRB() LANE2(ln_b); const unsigned tb_ = (4 * (ln_b >> 5) + ((ln_b & 15) >> 2)) * 128 + (((4 * cg + 2 * ((ln_b >> 4) & 1) + ((ln_b & 3) >> 1)) ^ (((ln_b & 15) >> 3) << 2)) << 4) + 8 * (ln_b & 1); \
        lptr trB0 = L + tb_ + cg * 4096, trB1 = L + (tb_ ^ 64u) + cg * 4096
#define DEF_XCH() lptr kp = L + AUX + (rg == grp ? 512 + cg * 2048 : 12800) + voffS; const int kstep = rg == grp ? 4096 : 0; lptr xw = L + wave * 4096 + voffS, xrd = L + (wave ^ 4) * 4096 + voffS
        bf16x8 ho[12], hp[12]; f32x16 bcc[6]; f32x4 xv0, xv1, xv2, xv3; unsigned hw0, hw1, hw2, hw3; float rl, z0, z1, z2, z3, z4, z5, m0, m1, m2, m3, m4, m5, n0, n1, n2, n3, n4, n5;
        const unsigned voffT = (rg * 32 + l31) * 8 + h * 3072;
#define FS11() do { FS11A(); rl = rr * 1.44269504088896341f; PIN(rl); *(LAS unsigned*)(L + AUX + (rg == grp ? 0 : 128) + l31 * 4) = stw; } while (0)
    ALD1(ep, 0u, ws + WS_EPOCH);
    BIAS_LOADS();
    GL(gw1, kw0 + 0, voffK1, 0);
    GL(gx, kx0 + 128, voffR, 106496);
    GL(gx, kx0 + 73856, voffR, 131072);
    GL(gw1, kw0 + 589824, voffK1, 8192);
    GL(gx, kx0 + 256, voffR, 114688);
    GL(gx, kx0 + 73984, voffR, 139264);
    GL(gw1, kw0 + 1179648, voffK1, 16384);
    asm volatile("s_waitcnt vmcnt(6) lgkmcnt(0)\n\ts_barrier" ::: "memory"); SBAR();
    rx0 += 98304; PIN(rx0); xf[0] = ldb128(rx0 + 0);
    wf0 = trfrag(trA + 0, 512);
    rx1 += 98304; PIN(rx1); xf[1] = ldb128(rx1 + 0);
    SBAR();
    GL(gx, kx1 + 0, voffR, 24576);
    SBAR();
    wf1 = trfrag(trA + 2048, 512);
    rx2 += 98304; PIN(rx2); xf[2] = ldb128(rx2 + 0);
    wf2 = trfrag(trA + 4096, 512);
    SBAR();
    GL(gx, kx1 + 73728, voffR, 49152);
    SBAR();
    rx3 += 98304; PIN(rx3); xf[3] = ldb128(rx3 + 0);
    wf3 = trfrag(trA + 6144, 512);
    cf0 = trfrag(trCS + 0, 512);
    SBAR();
    GL(gw1, kw1 + 0, voffK1, 73728);
    SBAR();
    GL(gx, kx1 + 128, voffR, 32768);
    SBAR();
    asm volatile("; VW %0" : "+v"(ep)); GOT_EPOCH();
    STAT_PART(0, 4); SBAR();
    asm volatile("s_waitcnt vmcnt(7) lgkmcnt(0)\n\ts_barrier" ::: "memory"); SBAR();
    acc0 = MFMA(wf0, xf[0], zero16);
    SBAR();
    xf[4] = ldb128(rx0 + 8192);
    wf4 = trfrag(trA + 8192, 512);
    xf[5] = ldb128(rx1 + 8192);
    SBAR();
    GL(gx, kx1 + 73856, voffR, 57344);
    SBAR();
    acc0 = MFMA(wf1, xf[1], acc0);
    SBAR();
    wf5 = trfrag(trA + 10240, 512);
    xf[6] = ldb128(rx2 + 8192);
    wf6 = trfrag(trA + 12288, 512);
    SBAR();
    GL(gw1, kw1 + 589824, voffK1, 81920);
    SBAR();
    acc0 = MFMA(wf2, xf[2], acc0);
    SBAR();
    xf[7] = ldb128(rx3 + 8192);
    wf7 = trfrag(trA + 14336, 512);
    cf1 = trfrag(trCS + 8192, 512);
    SBAR();
    GL(gx, kx1 + 256, voffR, 40960);
    SBAR();
    acc0 = MFMA(wf3, xf[3], acc0);
    SBAR();
    GL(gx, kx1 + 73984, voffR, 65536);
    SBAR();
    csa = MFMA(ones8, cf0, zero16);
    SBAR();
    STAT_PART(4, 8); SBAR();
    asm volatile("s_waitcnt vmcnt(8) lgkmcnt(0)\n\ts_barrier" ::: "memory"); SBAR();
    acc0 = MFMA(wf4, xf[4], acc0);
    SBAR();
    xf[8] = ldb128(rx0 + 16384);
    wf8 = trfrag(trA + 16384, 512);
    xf[9] = ldb128(rx1 + 16384);
    SBAR();
    GL(gw1, kw1 + 1179648, voffK1, 90112);
    SBAR();
    acc0 = MFMA(wf5, xf[5], acc0);
    SBAR();
    wf9 = trfrag(trA + 18432, 512);
    xf[10] = ldb128(rx2 + 16384);
    wf10 = trfrag(trA + 20480, 512);
    SBAR();
    GL(gx, kx2 + 0, voffR, 98304);
    SBAR();
    acc0 = MFMA(wf6, xf[6], acc0);
    SBAR();
    xf[11] = ldb128(rx3 + 16384);
    wf11 = trfrag(trA + 22528, 512);
    cf0 = trfrag(trCS + 16384, 512);
    SBAR();
    GL(gx, kx2 + 73728, voffR, 122880);
    SBAR();
    acc0 = MFMA(wf7, xf[7], acc0);
    SBAR();
    GL(gw1, kw2 + 0, voffK1, 0);
    SBAR();
    csa = MFMA(ones8, cf1, csa);
    SBAR();
    STAT_PART(8, 12); SBAR();
    STAT_PUT();
    asm volatile("s_waitcnt vmcnt(10) lgkmcnt(0)\n\ts_barrier" ::: "memory"); SBAR();
    acc0 = MFMA(wf8, xf[8], acc0);
    SBAR();
    rx0 += -73728; PIN(rx0); xf[12] = ldb128(rx0 + 0);
    trA += 73728; PIN(trA); wf0 = trfrag(trA + 0, 512);
    rx1 += -73728; PIN(rx1); xf[13] = ldb128(rx1 + 0);
    SBAR();
    GL(gx, kx2 + 128, voffR, 106496);
    SBAR();
    acc0 = MFMA(wf9, xf[9], acc0);
    SBAR();
    wf1 = trfrag(trA + 2048, 512);
    rx2 += -73728; PIN(rx2); xf[14] = ldb128(rx2 + 0);
    wf2 = trfrag(trA + 4096, 512);
    SBAR();
    GL(gx, kx2 + 73856, voffR, 131072);
    SBAR();
    acc0 = MFMA(wf10, xf[10], acc0);
    SBAR();
    rx3 += -73728; PIN(rx3); xf[15] = ldb128(rx3 + 0);
    wf3 = trfrag(trA + 6144, 512);
    trCS += 73728; PIN(trCS); cf1 = trfrag(trCS + 0, 512);
    SBAR();
    GL(gw1, kw2 + 589824, voffK1, 8192);
    SBAR();
    acc0 = MFMA(wf11, xf[11], acc0);
    SBAR();
    GL(gx, kx2 + 256, voffR, 114688);
    SBAR();
    csa = MFMA(ones8, cf0, csa);
    SBAR();
    asm volatile("s_waitcnt vmcnt(11) lgkmcnt(0)\n\ts_barrier" ::: "memory"); SBAR();
    acc0 = MFMA(wf0, xf[12], acc0);
    SBAR();
    xf[16] = ldb128(rx0 + 8192);
    wf4 = trfrag(trA + 8192, 512);
    xf[17] = ldb128(rx1 + 8192);
    SBAR();
    GL(gx, kx2 + 73984, voffR, 139264);
    SBAR();
    acc0 = MFMA(wf1, xf[13], acc0);
    SBAR();
    wf5 = trfrag(trA + 10240, 512);
    xf[18] = ldb128(rx2 + 8192);
    wf6 = trfrag(trA + 12288, 512);
    SBAR();
    GL(gw1, kw2 + 1179648, voffK1, 16384);
    SBAR();
    acc0 = MFMA(wf2, xf[14], acc0);
    SBAR();
    xf[19] = ldb128(rx3 + 8192);
    wf7 = trfrag(trA + 14336, 512);
    cf0 = trfrag(trCS + 8192, 512);
    SBAR();
    GL(gw1, kw0 + 128, voffK1, 24576);
    SBAR();
    acc0 = MFMA(wf3, xf[15], acc0);
    SBAR();
    csa = MFMA(ones8, cf1, csa);
    SBAR();
    VWAIT2(27, bv0, bv1);
    SBAR();
    asm volatile("s_waitcnt vmcnt(11) lgkmcnt(0)\n\ts_barrier" ::: "memory"); SBAR();
    acc0 = MFMA(wf4, xf[16], acc0);
    SBAR();
    xf[20] = ldb128(rx0 + 16384);
    wf8 = trfrag(trA + 16384, 512);
    xf[21] = ldb128(rx1 + 16384);
    SBAR();
    GL(gw1, kw0 + 589952, voffK1, 32768);
    SBAR();
    acc0 = MFMA(wf5, xf[17], acc0);
    SBAR();
    wf9 = trfrag(trA + 18432, 512);
    xf[22] = ldb128(rx2 + 16384);
    wf10 = trfrag(trA + 20480, 512);
    SBAR();
    acc0 = MFMA(wf6, xf[18], acc0);
    SBAR();
    xf[23] = ldb128(rx3 + 16384);
    wf11 = trfrag(trA + 22528, 512);
    cf1 = trfrag(trCS + 16384, 512);
    SBAR();
    acc0 = MFMA(wf7, xf[19], acc0);
    SBAR();
    csa = MFMA(ones8, cf0, csa);
    SBAR();
    asm volatile("s_waitcnt vmcnt(9) lgkmcnt(0)\n\ts_barrier" ::: "memory"); SBAR();
    acc0 = MFMA(wf8, xf[20], acc0);
    SBAR();
    rx0 += 73728; PIN(rx0); xf[24] = ldb128(rx0 + 0);
    trA += -73728; PIN(trA); wf0 = trfrag(trA + 0, 512);
    rx1 += 73728; PIN(rx1); xf[25] = ldb128(rx1 + 0);
    SBAR();
    GL(gw1, kw0 + 1179776, voffK1, 40960);
    SBAR();
    acc0 = MFMA(wf9, xf[21], acc0);
    SBAR();
    wf1 = trfrag(trA + 2048, 512);
    rx2 += 73728; PIN(rx2); xf[26] = ldb128(rx2 + 0);
    wf2 = trfrag(trA + 4096, 512);
    SBAR();
    GL(gw1, kw1 + 128, voffK1, 49152);
    SBAR();
    acc0 = MFMA(wf10, xf[22], acc0);
    SBAR();
    rx3 += 73728; PIN(rx3); xf[27] = ldb128(rx3 + 0);
    wf3 = trfrag(trA + 6144, 512);
    trCS += -73728; PIN(trCS); cf0 = trfrag(trCS + 0, 512);
    SBAR();
    GL(gw1, kw1 + 589952, voffK1, 57344);
    SBAR();
    acc0 = MFMA(wf11, xf[23], acc0);
    SBAR();
    GL(gw1, kw1 + 1179776, voffK1, 65536);
    SBAR();
    csa = MFMA(ones8, cf1, csa);
    SBAR();
    asm volatile("s_waitcnt vmcnt(9) lgkmcnt(0)\n\ts_barrier" ::: "memory"); SBAR();
    acc0 = MFMA(wf0, xf[24], acc0);
    SBAR();
    xf[28] = ldb128(rx0 + 8192);
    wf4 = trfrag(trA + 8192, 512);
    xf[29] = ldb128(rx1 + 8192);
    SBAR();
    GL(gw1, kw2 + 128, voffK1, 73728);
    SBAR();
    acc0 = MFMA(wf1, xf[25], acc0);
    SBAR();
    wf5 = trfrag(trA + 10240, 512);
    xf[30] = ldb128(rx2 + 8192);
    wf6 = trfrag(trA + 12288, 512);
    SBAR();
    GL(gw1, kw2 + 589952, voffK1, 81920);
    SBAR();
    acc0 = MFMA(wf2, xf[26], acc0);
    SBAR();
    xf[31] = ldb128(rx3 + 8192);
    wf7 = trfrag(trA + 14336, 512);
    cf1 = trfrag(trCS + 8192, 512);
    SBAR();
    GL(gw1, kw2 + 1179776, voffK1, 90112);
    SBAR();
    acc0 = MFMA(wf3, xf[27], acc0);
    SBAR();
    GL(gw1, kw0 + 256, voffK1, 98304);
    SBAR();
    csa = MFMA(ones8, cf0, csa);
    SBAR();
    asm volatile("s_waitcnt vmcnt(10) lgkmcnt(0)\n\ts_barrier" ::: "memory"); SBAR();
    acc0 = MFMA(wf4, xf[28], acc0);
    SBAR();
    cf0 = trfrag(trCS + 16384, 512);
    xf[32] = ldb128(rx0 + 16384);
    wf8 = trfrag(trA + 16384, 512);
    SBAR();
    GL(gw1, kw0 + 590080, voffK1, 106496);
    SBAR();
    acc0 = MFMA(wf5, xf[29], acc0);
    SBAR();
    xf[33] = ldb128(rx1 + 16384);
    wf9 = trfrag(trA + 18432, 512);
    xf[34] = ldb128(rx2 + 16384);
    SBAR();
    acc0 = MFMA(wf6, xf[30], acc0);
    SBAR();
    wf10 = trfrag(trA + 20480, 512);
    xf[35] = ldb128(rx3 + 16384);
    wf11 = trfrag(trA + 22528, 512);
    SBAR();
    acc0 = MFMA(wf7, xf[31], acc0);
    SBAR();
    csa = MFMA(ones8, cf1, csa);
    SBAR();
    ALD2(sg0, voffT, gps); ALD2(sg1, voffT, gps + 1024); ALD2(cv, voffT, gps + 2048);
    SBAR();
    csa = MFMA(ones8, cf0, csa); SBAR(); stf32(L + AUX + 2048 + voffC + rg * 4, csa[0]); SBAR();
    asm volatile("s_waitcnt vmcnt(8) lgkmcnt(0)\n\ts_barrier" ::: "memory"); SBAR();
    wf0 = trfrag(trA + 24576, 512);
    wf1 = trfrag(trA + 26624, 512);
    wf2 = trfrag(trA + 28672, 512);
    SBAR();
    csv = ldf4(L + AUX + 2048 + voffC);
    acc0 = MFMA(wf8, xf[32], acc0);
    SBAR();
    acc0 = MFMA(wf9, xf[33], acc0);
    SBAR();
    acc0 = MFMA(wf10, xf[34], acc0);
    SBAR();
    cor = mkcorr((csv[0] + csv[1]) + (csv[2] + csv[3]), bv0, 0u); PIN(cor);
    SBAR();
    acc0 = MFMA(wf11, xf[35], acc0);
    SBAR();
    acc1 = MFMA(wf0, xf[0], zero16);
    SBAR();
    wf3 = trfrag(trA + 30720, 512);
    SBAR();
    DEF_PRS();
    SBAR();
    GL(gw1, kw0 + 1179904, voffK1, 114688);
    SBAR();
    acc1 = MFMA(wf1, xf[1], acc1);
    SBAR();
    wf4 = trfrag(trA + 32768, 512);
    SBAR();
    acc1 = MFMA(wf2, xf[2], acc1);
    SBAR();
    wf5 = trfrag(trA + 34816, 512);
    SBAR();
    VWAIT3(1, sg0, sg1, cv); CHK_STAT();
    SBAR();
    GL(gw1, kw1 + 256, voffK1, 122880);
    SBAR();
    acc1 = MFMA(wf3, xf[3], acc1);
    SBAR();
    wf6 = trfrag(trA + 36864, 512);
    SBAR();
    FS1();
    SBAR();
    FS2();
    SBAR();
    acc1 = MFMA(wf4, xf[4], acc1);
    SBAR();
    wf7 = trfrag(trA + 38912, 512);
    SBAR();
    FS3();
    SBAR();
    FS4();
    SBAR();
    GL(gw1, kw1 + 590080, voffK1, 131072);
    SBAR();
    acc1 = MFMA(wf5, xf[5], acc1);
    SBAR();
    wf8 = trfrag(trA + 40960, 512);
    SBAR();
    FS5();
    SBAR();
    FS6();
    SBAR();
    acc1 = MFMA(wf6, xf[6], acc1);
    SBAR();
    wf9 = trfrag(trA + 43008, 512);
    SBAR();
    FS7();
    SBAR();
    FS8();
    SBAR();
    GL(gw1, kw1 + 1179904, voffK1, 139264);
    SBAR();
    acc1 = MFMA(wf7, xf[7], acc1);
    SBAR();
    wf10 = trfrag(trA + 45056, 512);
    SBAR();
    FS9();
    SBAR();
    FS10();
    SBAR();
    acc1 = MFMA(wf8, xf[8], acc1);
    SBAR();
    wf11 = trfrag(trA + 47104, 512);
    SBAR();
    FS11();
    SBAR();
    GL(gw1, kw2 + 256, voffK1, 0);
    SBAR();
    acc0 = MFMA(CORF, lnf, acc0);
    SBAR();
    acc1 = MFMA(wf9, xf[9], acc1);
    SBAR();
    trA += 49152; PIN(trA); wf0 = trfrag(trA + 0, 512);
    SBAR();
    PF1(0);
    SBAR();
    acc1 = MFMA(wf10, xf[10], acc1);
    SBAR();
    wf1 = trfrag(trA + 2048, 512);
    wf2 = trfrag(trA + 4096, 512);
    SBAR();
    z0 = acc0[0] * rl; PIN(z0);
    SBAR();
    GL(gw1, kw2 + 590080, voffK1, 8192);
    SBAR();
    acc1 = MFMA(wf11, xf[11], acc1);
    SBAR();
    asm volatile("s_waitcnt vmcnt(12)\n\ts_barrier" ::: "memory"); SBAR();
    acc1 = MFMA(wf0, xf[12], acc1);
    SBAR();
    wf3 = trfrag(trA + 6144, 512);
    SBAR();
    PF1(1);
    SBAR();
    m0 = __builtin_amdgcn_exp2f(z0); PIN(m0);
    SBAR();
    z1 = acc0[1] * rl; PIN(z1);
    SBAR();
    acc1 = MFMA(wf1, xf[13], acc1);
    SBAR();
    wf4 = trfrag(trA + 8192, 512);
    SBAR();
    n0 = FMA(m0, 0.5f, 1.f); PIN(n0);
    SBAR();
    m1 = __builtin_amdgcn_exp2f(z1); PIN(m1);
    SBAR();
    z2 = acc0[2] * rl; PIN(z2);
    SBAR();
    GL(gw1, kw2 + 1179904, voffK1, 16384);
    SBAR();
    acc1 = MFMA(wf2, xf[14], acc1);
    SBAR();
    wf5 = trfrag(trA + 10240, 512);
    SBAR();
    PF1(2);
    SBAR();
    m0 = FMA(m0, n0, 1.f); PIN(m0);
    SBAR();
    n1 = FMA(m1, 0.5f, 1.f); PIN(n1);
    SBAR();
    m2 = __builtin_amdgcn_exp2f(z2); PIN(m2);
    SBAR();
    acc1 = MFMA(wf3, xf[15], acc1);
    SBAR();
    wf6 = trfrag(trA + 12288, 512);
    SBAR();
    m0 = __builtin_amdgcn_rcpf(m0); PIN(m0);
    SBAR();
    m1 = FMA(m1, n1, 1.f); PIN(m1);
    SBAR();
    n2 = FMA(m2, 0.5f, 1.f); PIN(n2);
    SBAR();
    z3 = acc0[3] * rl; PIN(z3);
    SBAR();
    acc1 = MFMA(wf4, xf[16], acc1);
    SBAR();
    wf7 = trfrag(trA + 14336, 512);
    SBAR();
    PFB();
    SBAR();
    z0 = FMA(-z0, m0, z0); PIN(z0);
    SBAR();
    m1 = __builtin_amdgcn_rcpf(m1); PIN(m1);
    SBAR();
    m2 = FMA(m2, n2, 1.f); PIN(m2);
    SBAR();
    m3 = __builtin_amdgcn_exp2f(z3); PIN(m3);
    SBAR();
    z4 = acc0[4] * rl; PIN(z4);
    SBAR();
    GL(gw1, kw0 + 384, voffK1, 24576);
    SBAR();
    acc1 = MFMA(wf5, xf[17], acc1);
    SBAR();
    wf8 = trfrag(trA + 16384, 512);
    SBAR();
    z1 = FMA(-z1, m1, z1); PIN(z1);
    SBAR();
    m2 = __builtin_amdgcn_rcpf(m2); PIN(m2);
    SBAR();
    n3 = FMA(m3, 0.5f, 1.f); PIN(n3);
    SBAR();
    m4 = __builtin_amdgcn_exp2f(z4); PIN(m4);
    SBAR();
    acc1 = MFMA(wf6, xf[18], acc1);
    SBAR();
    wf9 = trfrag(trA + 18432, 512);
    SBAR();
    hw0 = cvtpk(z0, z1); PIN(hw0);
    SBAR();
    z2 = FMA(-z2, m2, z2); PIN(z2);
    SBAR();
    m3 = FMA(m3, n3, 1.f); PIN(m3);
    SBAR();
    n4 = FMA(m4, 0.5f, 1.f); PIN(n4);
    SBAR();
    z5 = acc0[5] * rl; PIN(z5);
    SBAR();
    acc1 = MFMA(wf7, xf[19], acc1);
    SBAR();
    wf10 = trfrag(trA + 20480, 512);
    SBAR();
    m3 = __builtin_amdgcn_rcpf(m3); PIN(m3);
    SBAR();
    m4 = FMA(m4, n4, 1.f); PIN(m4);
    SBAR();
    m5 = __builtin_amdgcn_exp2f(z5); PIN(m5);
    SBAR();
    z0 = acc0[6] * rl; PIN(z0);
    SBAR();
    GL(gw1, kw0 + 590208, voffK1, 32768);
    SBAR();
    acc1 = MFMA(wf8, xf[20], acc1);
    SBAR();
    wf11 = trfrag(trA + 22528, 512);
    SBAR();
    z3 = FMA(-z3, m3, z3); PIN(z3);
    SBAR();
    m4 = __builtin_amdgcn_rcpf(m4); PIN(m4);
    SBAR();
    n5 = FMA(m5, 0.5f, 1.f); PIN(n5);
    SBAR();
    m0 = __builtin_amdgcn_exp2f(z0); PIN(m0);
    SBAR();
    z1 = acc0[7] * rl; PIN(z1);
    SBAR();
    acc1 = MFMA(wf9, xf[21], acc1);
    SBAR();
    wf0 = trfrag(trA + 24576, 512);
    SBAR();
    ALD4(cor, voffC, gco + 1024);
    SBAR();
    hw1 = cvtpk(z2, z3); PIN(hw1);
    SBAR();
    z4 = FMA(-z4, m4, z4); PIN(z4);
    SBAR();
    m5 = FMA(m5, n5, 1.f); PIN(m5);
    SBAR();
    n0 = FMA(m0, 0.5f, 1.f); PIN(n0);
    SBAR();
    m1 = __builtin_amdgcn_exp2f(z1); PIN(m1);
    SBAR();
    acc1 = MFMA(wf10, xf[22], acc1);
    SBAR();
    wf1 = trfrag(trA + 26624, 512);
    wf2 = trfrag(trA + 28672, 512);
    SBAR();
    m5 = __builtin_amdgcn_rcpf(m5); PIN(m5);
    SBAR();
    m0 = FMA(m0, n0, 1.f); PIN(m0);
    SBAR();
    n1 = FMA(m1, 0.5f, 1.f); PIN(n1);
    SBAR();
    z2 = acc0[8] * rl; PIN(z2);
    SBAR();
    GL(gw1, kw0 + 1180032, voffK1, 40960);
    SBAR();
    acc1 = MFMA(wf11, xf[23], acc1);
    SBAR();
    asm volatile("s_waitcnt vmcnt(14)\n\ts_barrier" ::: "memory"); SBAR();
    acc1 = MFMA(wf0, xf[24], acc1);
    SBAR();
    wf3 = trfrag(trA + 30720, 512);
    SBAR();
    z5 = FMA(-z5, m5, z5); PIN(z5);
    SBAR();
    m0 = __builtin_amdgcn_rcpf(m0); PIN(m0);
    SBAR();
    m1 = FMA(m1, n1, 1.f); PIN(m1);
    SBAR();
    m2 = __builtin_amdgcn_exp2f(z2); PIN(m2);
    SBAR();
    z3 = acc0[9] * rl; PIN(z3);
    SBAR();
    acc1 = MFMA(wf1, xf[25], acc1);
    SBAR();
    wf4 = trfrag(trA + 32768, 512);
    SBAR();
    hw2 = cvtpk(z4, z5); PIN(hw2);
    SBAR();
    z0 = FMA(-z0, m0, z0); PIN(z0);
    SBAR();
    m1 = __builtin_amdgcn_rcpf(m1); PIN(m1);
    SBAR();
    n2 = FMA(m2, 0.5f, 1.f); PIN(n2);
    SBAR();
    m3 = __builtin_amdgcn_exp2f(z3); PIN(m3);
    SBAR();
    GL(gw1, kw1 + 384, voffK1, 49152);
    SBAR();
    acc1 = MFMA(wf2, xf[26], acc1);
    SBAR();
    wf5 = trfrag(trA + 34816, 512);
    SBAR();
    z1 = FMA(-z1, m1, z1); PIN(z1);
    SBAR();
    m2 = FMA(m2, n2, 1.f); PIN(m2);
    SBAR();
    n3 = FMA(m3, 0.5f, 1.f); PIN(n3);
    SBAR();
    z4 = acc0[10] * rl; PIN(z4);
    SBAR();
    acc1 = MFMA(wf3, xf[27], acc1);
    SBAR();
    wf6 = trfrag(trA + 36864, 512);
    SBAR();
    hw3 = cvtpk(z0, z1); PIN(hw3); ho[0] = __builtin_bit_cast(bf16x8, (u32x4){hw0, hw1, hw2, hw3}); PIN(ho[0]);
    SBAR();
    m2 = __builtin_amdgcn_rcpf(m2); PIN(m2);
    SBAR();
    m3 = FMA(m3, n3, 1.f); PIN(m3);
    SBAR();
    m4 = __builtin_amdgcn_exp2f(z4); PIN(m4);
    SBAR();
    z5 = acc0[11] * rl; PIN(z5);
    SBAR();
    acc1 = MFMA(wf4, xf[28], acc1);
    SBAR();
    wf7 = trfrag(trA + 38912, 512);
    SBAR();
    z2 = FMA(-z2, m2, z2); PIN(z2);
    SBAR();
    m3 = __builtin_amdgcn_rcpf(m3); PIN(m3);
    SBAR();
    n4 = FMA(m4, 0.5f, 1.f); PIN(n4);
    SBAR();
    m5 = __builtin_amdgcn_exp2f(z5); PIN(m5);
    SBAR();
    z0 = acc0[12] * rl; PIN(z0);
    SBAR();
    sth(ho[0], 0);
    SBAR();
    acc1 = MFMA(wf5, xf[29], acc1);
    SBAR();
    wf8 = trfrag(trA + 40960, 512);
    SBAR();
    VWAIT1(3, cor); CHK_COR(cor, 1);
    SBAR();
    z3 = FMA(-z3, m3, z3); PIN(z3);
    SBAR();
    m4 = FMA(m4, n4, 1.f); PIN(m4);
    SBAR();
    n5 = FMA(m5, 0.5f, 1.f); PIN(n5);
    SBAR();
    m0 = __builtin_amdgcn_exp2f(z0); PIN(m0);
    SBAR();
    GL(gw1, kw1 + 590208, voffK1, 57344);
    SBAR();
    acc1 = MFMA(wf6, xf[30], acc1);
    SBAR();
    wf9 = trfrag(trA + 43008, 512);
    SBAR();
    hw0 = cvtpk(z2, z3); PIN(hw0);
    SBAR();
    m4 = __builtin_amdgcn_rcpf(m4); PIN(m4);
    SBAR();
    m5 = FMA(m5, n5, 1.f); PIN(m5);
    SBAR();
    n0 = FMA(m0, 0.5f, 1.f); PIN(n0);
    SBAR();
    z1 = acc0[13] * rl; PIN(z1);
    SBAR();
    acc1 = MFMA(wf7, xf[31], acc1);
    SBAR();
    wf10 = trfrag(trA + 45056, 512);
    SBAR();
    z4 = FMA(-z4, m4, z4); PIN(z4);
    SBAR();
    m5 = __builtin_amdgcn_rcpf(m5); PIN(m5);
    SBAR();
    m0 = FMA(m0, n0, 1.f); PIN(m0);
    SBAR();
    m1 = __builtin_amdgcn_exp2f(z1); PIN(m1);
    SBAR();
    z2 = acc0[14] * rl; PIN(z2);
    SBAR();
    acc1 = MFMA(CORF, lnf, acc1);
    SBAR();
    acc1 = MFMA(wf8, xf[32], acc1);
    SBAR();
    wf11 = trfrag(trA + 47104, 512);
    SBAR();
    z5 = FMA(-z5, m5, z5); PIN(z5);
    SBAR();
    m0 = __builtin_amdgcn_rcpf(m0); PIN(m0);
    SBAR();
    n1 = FMA(m1, 0.5f, 1.f); PIN(n1);
    SBAR();
    m2 = __builtin_amdgcn_exp2f(z2); PIN(m2);
    SBAR();
    acc1 = MFMA(wf9, xf[33], acc1);
    SBAR();
    trA += 49152; PIN(trA); wf0 = trfrag(trA + 0, 512);
    SBAR();
    hw1 = cvtpk(z4, z5); PIN(hw1);
    SBAR();
    z0 = FMA(-z0, m0, z0); PIN(z0);
    SBAR();
    m1 = FMA(m1, n1, 1.f); PIN(m1);
    SBAR();
    n2 = FMA(m2, 0.5f, 1.f); PIN(n2);
    SBAR();
    z3 = acc0[15] * rl; PIN(z3);
    SBAR();
    GL(gw1, kw1 + 1180032, voffK1, 65536);
    SBAR();
    acc1 = MFMA(wf10, xf[34], acc1);
    SBAR();
    wf1 = trfrag(trA + 2048, 512);
    wf2 = trfrag(trA + 4096, 512);
    SBAR();
    m1 = __builtin_amdgcn_rcpf(m1); PIN(m1);
    SBAR();
    m2 = FMA(m2, n2, 1.f); PIN(m2);
    SBAR();
    m3 = __builtin_amdgcn_exp2f(z3); PIN(m3);
    SBAR();
    acc1 = MFMA(wf11, xf[35], acc1);
    SBAR();
    asm volatile("s_waitcnt vmcnt(15)\n\ts_barrier" ::: "memory"); SBAR();
    acc0 = MFMA(wf0, xf[0], zero16);
    SBAR();
    wf3 = trfrag(trA + 6144, 512);
    SBAR();
    z1 = FMA(-z1, m1, z1); PIN(z1);
    SBAR();
    m2 = __builtin_amdgcn_rcpf(m2); PIN(m2);
    SBAR();
    n3 = FMA(m3, 0.5f, 1.f); PIN(n3);
    SBAR();
    acc0 = MFMA(wf1, xf[1], acc0);
    SBAR();
    wf4 = trfrag(trA + 8192, 512);
    SBAR();
    hw2 = cvtpk(z0, z1); PIN(hw2);
    SBAR();
    z2 = FMA(-z2, m2, z2); PIN(z2);
    SBAR();
    m3 = FMA(m3, n3, 1.f); PIN(m3);
    SBAR();
    z4 = acc1[0] * rl; PIN(z4);
    SBAR();
    GL(gw1, kw2 + 384, voffK1, 73728);
    SBAR();
    acc0 = MFMA(wf2, xf[2], acc0);
    SBAR();
    wf5 = trfrag(trA + 10240, 512);
    SBAR();
    m3 = __builtin_amdgcn_rcpf(m3); PIN(m3);
    SBAR();
    m4 = __builtin_amdgcn_exp2f(z4); PIN(m4);
    SBAR();
    acc0 = MFMA(wf3, xf[3], acc0);
    SBAR();
    wf6 = trfrag(trA + 12288, 512);
    SBAR();
    z3 = FMA(-z3, m3, z3); PIN(z3);
    SBAR();
    n4 = FMA(m4, 0.5f, 1.f); PIN(n4);
    SBAR();
    z5 = acc1[1] * rl; PIN(z5);
    SBAR();
    acc0 = MFMA(wf4, xf[4], acc0);
    SBAR();
    wf7 = trfrag(trA + 14336, 512);
    SBAR();
    hw3 = cvtpk(z2, z3); PIN(hw3); ho[1] = __builtin_bit_cast(bf16x8, (u32x4){hw0, hw1, hw2, hw3}); PIN(ho[1]);
    SBAR();
    m4 = FMA(m4, n4, 1.f); PIN(m4);
    SBAR();
    m5 = __builtin_amdgcn_exp2f(z5); PIN(m5);
    SBAR();
    acc0 = MFMA(wf5, xf[5], acc0);
    SBAR();
    wf8 = trfrag(trA + 16384, 512);
    SBAR();
    m4 = __builtin_amdgcn_rcpf(m4); PIN(m4);
    SBAR();
    n5 = FMA(m5, 0.5f, 1.f); PIN(n5);
    SBAR();
    z0 = acc1[2] * rl; PIN(z0);
    SBAR();
    sth(ho[1], 1024);
    SBAR();
    GL(gw1, kw2 + 590208, voffK1, 81920);
    SBAR();
    acc0 = MFMA(wf6, xf[6], acc0);
    SBAR();
    wf9 = trfrag(trA + 18432, 512);
    SBAR();
    z4 = FMA(-z4, m4, z4); PIN(z4);
    SBAR();
    m5 = FMA(m5, n5, 1.f); PIN(m5);
    SBAR();
    m0 = __builtin_amdgcn_exp2f(z0); PIN(m0);
    SBAR();
    acc0 = MFMA(wf7, xf[7], acc0);
    SBAR();
    wf10 = trfrag(trA + 20480, 512);
    SBAR();
    m5 = __builtin_amdgcn_rcpf(m5); PIN(m5);
    SBAR();
    n0 = FMA(m0, 0.5f, 1.f); PIN(n0);
    SBAR();
    z1 = acc1[3] * rl; PIN(z1);
    SBAR();
    acc0 = MFMA(wf8, xf[8], acc0);
    SBAR();
    wf11 = trfrag(trA + 22528, 512);
    SBAR();
    z5 = FMA(-z5, m5, z5); PIN(z5);
    SBAR();
    m0 = FMA(m0, n0, 1.f); PIN(m0);
    SBAR();
    m1 = __builtin_amdgcn_exp2f(z1); PIN(m1);
    SBAR();
    acc0 = MFMA(wf9, xf[9], acc0);
    SBAR();
    wf0 = trfrag(trA + 24576, 512);
    SBAR();
    hw0 = cvtpk(z4, z5); PIN(hw0);
    SBAR();
    m0 = __builtin_amdgcn_rcpf(m0); PIN(m0);
    SBAR();
    n1 = FMA(m1, 0.5f, 1.f); PIN(n1);
    SBAR();
    z2 = acc1[4] * rl; PIN(z2);
    SBAR();
    GL(gw1, kw2 + 1180032, voffK1, 90112);
    SBAR();
    acc0 = MFMA(wf10, xf[10], acc0);
    SBAR();
    wf1 = trfrag(trA + 26624, 512);
    wf2 = trfrag(trA + 28672, 512);
    SBAR();
    z0 = FMA(-z0, m0, z0); PIN(z0);
    SBAR();
    m1 = FMA(m1, n1, 1.f); PIN(m1);
    SBAR();
    m2 = __builtin_amdgcn_exp2f(z2); PIN(m2);
    SBAR();
    acc0 = MFMA(wf11, xf[11], acc0);
    SBAR();
    asm volatile("s_waitcnt vmcnt(14)\n\ts_barrier" ::: "memory"); SBAR();
    acc0 = MFMA(wf0, xf[12], acc0);
    SBAR();
    wf3 = trfrag(trA + 30720, 512);
    SBAR();
    m1 = __builtin_amdgcn_rcpf(m1); PIN(m1);
    SBAR();
    n2 = FMA(m2, 0.5f, 1.f); PIN(n2);
    SBAR();
    z3 = acc1[5] * rl; PIN(z3);
    SBAR();
    acc0 = MFMA(wf1, xf[13], acc0);
    SBAR();
    wf4 = trfrag(trA + 32768, 512);
    SBAR();
    z1 = FMA(-z1, m1, z1); PIN(z1);
    SBAR();
    m2 = FMA(m2, n2, 1.f); PIN(m2);
    SBAR();
    m3 = __builtin_amdgcn_exp2f(z3); PIN(m3);
    SBAR();
    GL(gw1, kw0 + 512, voffK1, 98304);
    SBAR();
    acc0 = MFMA(wf2, xf[14], acc0);
    SBAR();
    wf5 = trfrag(trA + 34816, 512);
    SBAR();
    hw1 = cvtpk(z0, z1); PIN(hw1);
    SBAR();
    m2 = __builtin_amdgcn_rcpf(m2); PIN(m2);
    SBAR();
    n3 = FMA(m3, 0.5f, 1.f); PIN(n3);
    SBAR();
    z4 = acc1[6] * rl; PIN(z4);
    SBAR();
    acc0 = MFMA(wf3, xf[15], acc0);
    SBAR();
    wf6 = trfrag(trA + 36864, 512);
    SBAR();
    z2 = FMA(-z2, m2, z2); PIN(z2);
    SBAR();
    m3 = FMA(m3, n3, 1.f); PIN(m3);
    SBAR();
    m4 = __builtin_amdgcn_exp2f(z4); PIN(m4);
    SBAR();
    acc0 = MFMA(wf4, xf[16], acc0);
    SBAR();
    wf7 = trfrag(trA + 38912, 512);
    SBAR();
    ALD4(cor, voffC, gco + 2048);
    SBAR();
    m3 = __builtin_amdgcn_rcpf(m3); PIN(m3);
    SBAR();
    n4 = FMA(m4, 0.5f, 1.f); PIN(n4);
    SBAR();
    z5 = acc1[7] * rl; PIN(z5);
    SBAR();
    acc0 = MFMA(wf5, xf[17], acc0);
    SBAR();
    wf8 = trfrag(trA + 40960, 512);
    SBAR();
    z3 = FMA(-z3, m3, z3); PIN(z3);
    SBAR();
    m4 = FMA(m4, n4, 1.f); PIN(m4);
    SBAR();
    m5 = __builtin_amdgcn_exp2f(z5); PIN(m5);
    SBAR();
    GL(gw1, kw0 + 590336, voffK1, 106496);
    SBAR();
    acc0 = MFMA(wf6, xf[18], acc0);
    SBAR();
    wf9 = trfrag(trA + 43008, 512);
    SBAR();
    PF2(0);
    SBAR();
    hw2 = cvtpk(z2, z3); PIN(hw2);
    SBAR();
    m4 = __builtin_amdgcn_rcpf(m4); PIN(m4);
    SBAR();
    n5 = FMA(m5, 0.5f, 1.f); PIN(n5);
    SBAR();
    z0 = acc1[8] * rl; PIN(z0);
    SBAR();
    acc0 = MFMA(wf7, xf[19], acc0);
    SBAR();
    wf10 = trfrag(trA + 45056, 512);
    SBAR();
    z4 = FMA(-z4, m4, z4); PIN(z4);
    SBAR();
    m5 = FMA(m5, n5, 1.f); PIN(m5);
    SBAR();
    m0 = __builtin_amdgcn_exp2f(z0); PIN(m0);
    SBAR();
    acc0 = MFMA(wf8, xf[20], acc0);
    SBAR();
    wf11 = trfrag(trA + 47104, 512);
    SBAR();
    PF2(1);
    SBAR();
    m5 = __builtin_amdgcn_rcpf(m5); PIN(m5);
    SBAR();
    n0 = FMA(m0, 0.5f, 1.f); PIN(n0);
    SBAR();
    z1 = acc1[9] * rl; PIN(z1);
    SBAR();
    acc0 = MFMA(wf9, xf[21], acc0);
    SBAR();
    trA += -98304; PIN(trA); wf0 = trfrag(trA + 0, 512);
    SBAR();
    z5 = FMA(-z5, m5, z5); PIN(z5);
    SBAR();
    m0 = FMA(m0, n0, 1.f); PIN(m0);
    SBAR();
    m1 = __builtin_amdgcn_exp2f(z1); PIN(m1);
    SBAR();
    GL(gw1, kw0 + 1180160, voffK1, 114688);
    SBAR();
    acc0 = MFMA(wf10, xf[22], acc0);
    SBAR();
    wf1 = trfrag(trA + 2048, 512);
    wf2 = trfrag(trA + 4096, 512);
    SBAR();
    hw3 = cvtpk(z4, z5); PIN(hw3); ho[2] = __builtin_bit_cast(bf16x8, (u32x4){hw0, hw1, hw2, hw3}); PIN(ho[2]);
    SBAR();
    m0 = __builtin_amdgcn_rcpf(m0); PIN(m0);
    SBAR();
    n1 = FMA(m1, 0.5f, 1.f); PIN(n1);
    SBAR();
    z2 = acc1[10] * rl; PIN(z2);
    SBAR();
    acc0 = MFMA(wf11, xf[23], acc0);
    SBAR();
    asm volatile("s_waitcnt vmcnt(14)\n\ts_barrier" ::: "memory"); SBAR();
    acc0 = MFMA(wf0, xf[24], acc0);
    SBAR();
    wf3 = trfrag(trA + 6144, 512);
    SBAR();
    z0 = FMA(-z0, m0, z0); PIN(z0);
    SBAR();
    m1 = FMA(m1, n1, 1.f); PIN(m1);
    SBAR();
    m2 = __builtin_amdgcn_exp2f(z2); PIN(m2);
    SBAR();
    sth(ho[2], 2048);
    SBAR();
    acc0 = MFMA(wf1, xf[25], acc0);
    SBAR();
    wf4 = trfrag(trA + 8192, 512);
    SBAR();
    m1 = __builtin_amdgcn_rcpf(m1); PIN(m1);
    SBAR();
    n2 = FMA(m2, 0.5f, 1.f); PIN(n2);
    SBAR();
    z3 = acc1[11] * rl; PIN(z3);
    SBAR();
    GL(gw1, kw1 + 512, voffK1, 122880);
    SBAR();
    acc0 = MFMA(wf2, xf[26], acc0);
    SBAR();
    wf5 = trfrag(trA + 10240, 512);
    SBAR();
    z1 = FMA(-z1, m1, z1); PIN(z1);
    SBAR();
    m2 = FMA(m2, n2, 1.f); PIN(m2);
    SBAR();
    m3 = __builtin_amdgcn_exp2f(z3); PIN(m3);
    SBAR();
    acc0 = MFMA(wf3, xf[27], acc0);
    SBAR();
    wf6 = trfrag(trA + 12288, 512);
    SBAR();
    hw0 = cvtpk(z0, z1); PIN(hw0);
    SBAR();
    m2 = __builtin_amdgcn_rcpf(m2); PIN(m2);
    SBAR();
    n3 = FMA(m3, 0.5f, 1.f); PIN(n3);
    SBAR();
    z4 = acc1[12] * rl; PIN(z4);
    SBAR();
    acc0 = MFMA(wf4, xf[28], acc0);
    SBAR();
    wf7 = trfrag(trA + 14336, 512);
    SBAR();
    z2 = FMA(-z2, m2, z2); PIN(z2);
    SBAR();
    m3 = FMA(m3, n3, 1.f); PIN(m3);
    SBAR();
    m4 = __builtin_amdgcn_exp2f(z4); PIN(m4);
    SBAR();
    acc0 = MFMA(wf5, xf[29], acc0);
    SBAR();
    wf8 = trfrag(trA + 16384, 512);
    SBAR();
    VWAIT1(6, cor); CHK_COR(cor, 2);
    SBAR();
    m3 = __builtin_amdgcn_rcpf(m3); PIN(m3);
    SBAR();
    n4 = FMA(m4, 0.5f, 1.f); PIN(n4);
    SBAR();
    z5 = acc1[13] * rl; PIN(z5);
    SBAR();
    GL(gw1, kw1 + 590336, voffK1, 131072);
    SBAR();
    acc0 = MFMA(wf6, xf[30], acc0);
    SBAR();
    wf9 = trfrag(trA + 18432, 512);
    SBAR();
    z3 = FMA(-z3, m3, z3); PIN(z3);
    SBAR();
    m4 = FMA(m4, n4, 1.f); PIN(m4);
    SBAR();
    m5 = __builtin_amdgcn_exp2f(z5); PIN(m5);
    SBAR();
    acc0 = MFMA(wf7, xf[31], acc0);
    SBAR();
    wf10 = trfrag(trA + 20480, 512);
    SBAR();
    hw1 = cvtpk(z2, z3); PIN(hw1);
    SBAR();
    m4 = __builtin_amdgcn_rcpf(m4); PIN(m4);
    SBAR();
    n5 = FMA(m5, 0.5f, 1.f); PIN(n5);
    SBAR();
    z0 = acc1[14] * rl; PIN(z0);
    SBAR();
    acc0 = MFMA(CORF, lnf, acc0);
    SBAR();
    acc0 = MFMA(wf8, xf[32], acc0);
    SBAR();
    wf11 = trfrag(trA + 22528, 512);
    SBAR();
    z4 = FMA(-z4, m4, z4); PIN(z4);
    SBAR();
    m5 = FMA(m5, n5, 1.f); PIN(m5);
    SBAR();
    m0 = __builtin_amdgcn_exp2f(z0); PIN(m0);
    SBAR();
    acc0 = MFMA(wf9, xf[33], acc0);
    SBAR();
    wf0 = trfrag(trA + 24576, 512);
    SBAR();
    m5 = __builtin_amdgcn_rcpf(m5); PIN(m5);
    SBAR();
    n0 = FMA(m0, 0.5f, 1.f); PIN(n0);
    SBAR();
    z1 = acc1[15] * rl; PIN(z1);
    SBAR();
    GL(gw1, kw1 + 1180160, voffK1, 139264);
    SBAR();
    acc0 = MFMA(wf10, xf[34], acc0);
    SBAR();
    wf1 = trfrag(trA + 26624, 512);
    wf2 = trfrag(trA + 28672, 512);
    SBAR();
    z5 = FMA(-z5, m5, z5); PIN(z5);
    SBAR();
    m0 = FMA(m0, n0, 1.f); PIN(m0);
    SBAR();
    m1 = __builtin_amdgcn_exp2f(z1); PIN(m1);
    SBAR();
    acc0 = MFMA(wf11, xf[35], acc0);
    SBAR();
    asm volatile("s_waitcnt vmcnt(14)\n\ts_barrier" ::: "memory"); SBAR();
    acc1 = MFMA(wf0, xf[0], zero16);
    SBAR();
    wf3 = trfrag(trA + 30720, 512);
    SBAR();
    hw2 = cvtpk(z4, z5); PIN(hw2);
    SBAR();
    m0 = __builtin_amdgcn_rcpf(m0); PIN(m0);
    SBAR();
    n1 = FMA(m1, 0.5f, 1.f); PIN(n1);
    SBAR();
    acc1 = MFMA(wf1, xf[1], acc1);
    SBAR();
    wf4 = trfrag(trA + 32768, 512);
    SBAR();
    z0 = FMA(-z0, m0, z0); PIN(z0);
    SBAR();
    m1 = FMA(m1, n1, 1.f); PIN(m1);
    SBAR();
    z2 = acc0[0] * rl; PIN(z2);
    SBAR();
    GL(gw1, kw2 + 512, voffK1, 0);
    SBAR();
    acc1 = MFMA(wf2, xf[2], acc1);
    SBAR();
    wf5 = trfrag(trA + 34816, 512);
    SBAR();
    m1 = __builtin_amdgcn_rcpf(m1); PIN(m1);
    SBAR();
    m2 = __builtin_amdgcn_exp2f(z2); PIN(m2);
    SBAR();
    acc1 = MFMA(wf3, xf[3], acc1);
    SBAR();
    wf6 = trfrag(trA + 36864, 512);
    SBAR();
    z1 = FMA(-z1, m1, z1); PIN(z1);
    SBAR();
    n2 = FMA(m2, 0.5f, 1.f); PIN(n2);
    SBAR();
    z3 = acc0[1] * rl; PIN(z3);
    SBAR();
    acc1 = MFMA(wf4, xf[4], acc1);
    SBAR();
    wf7 = trfrag(trA + 38912, 512);
    SBAR();
    hw3 = cvtpk(z0, z1); PIN(hw3); ho[3] = __builtin_bit_cast(bf16x8, (u32x4){hw0, hw1, hw2, hw3}); PIN(ho[3]);
    SBAR();
    m2 = FMA(m2, n2, 1.f); PIN(m2);
    SBAR();
    m3 = __builtin_amdgcn_exp2f(z3); PIN(m3);
    SBAR();
    acc1 = MFMA(wf5, xf[5], acc1);
    SBAR();
    wf8 = trfrag(trA + 40960, 512);
    SBAR();
    m2 = __builtin_amdgcn_rcpf(m2); PIN(m2);
    SBAR();
    n3 = FMA(m3, 0.5f, 1.f); PIN(n3);
    SBAR();
    z4 = acc0[2] * rl; PIN(z4);
    SBAR();
    sth(ho[3], 3072);
    SBAR();
    GL(gw1, kw2 + 590336, voffK1, 8192);
    SBAR();
    acc1 = MFMA(wf6, xf[6], acc1);
    SBAR();
    wf9 = trfrag(trA + 43008, 512);
    SBAR();
    z2 = FMA(-z2, m2, z2); PIN(z2);
    SBAR();
    m3 = FMA(m3, n3, 1.f); PIN(m3);
    SBAR();
    m4 = __builtin_amdgcn_exp2f(z4); PIN(m4);
    SBAR();
    acc1 = MFMA(wf7, xf[7], acc1);
    SBAR();
    wf10 = trfrag(trA + 45056, 512);
    SBAR();
    m3 = __builtin_amdgcn_rcpf(m3); PIN(m3);
    SBAR();
    n4 = FMA(m4, 0.5f, 1.f); PIN(n4);
    SBAR();
    z5 = acc0[3] * rl; PIN(z5);
    SBAR();
    acc1 = MFMA(wf8, xf[8], acc1);
    SBAR();
    wf11 = trfrag(trA + 47104, 512);
    SBAR();
    z3 = FMA(-z3, m3, z3); PIN(z3);
    SBAR();
    m4 = FMA(m4, n4, 1.f); PIN(m4);
    SBAR();
    m5 = __builtin_amdgcn_exp2f(z5); PIN(m5);
    SBAR();
    acc1 = MFMA(wf9, xf[9], acc1);
    SBAR();
    trA += 49152; PIN(trA); wf0 = trfrag(trA + 0, 512);
    SBAR();
    hw0 = cvtpk(z2, z3); PIN(hw0);
    SBAR();
    m4 = __builtin_amdgcn_rcpf(m4); PIN(m4);
    SBAR();
    n5 = FMA(m5, 0.5f, 1.f); PIN(n5);
    SBAR();
    z0 = acc0[4] * rl; PIN(z0);
    SBAR();
    GL(gw1, kw2 + 1180160, voffK1, 16384);
    SBAR();
    acc1 = MFMA(wf10, xf[10], acc1);
    SBAR();
    wf1 = trfrag(trA + 2048, 512);
    wf2 = trfrag(trA + 4096, 512);
    SBAR();
    z4 = FMA(-z4, m4, z4); PIN(z4);
    SBAR();
    m5 = FMA(m5, n5, 1.f); PIN(m5);
    SBAR();
    m0 = __builtin_amdgcn_exp2f(z0); PIN(m0);
    SBAR();
    acc1 = MFMA(wf11, xf[11], acc1);
    SBAR();
    asm volatile("s_waitcnt vmcnt(14)\n\ts_barrier" ::: "memory"); SBAR();
    acc1 = MFMA(wf0, xf[12], acc1);
    SBAR();
    wf3 = trfrag(trA + 6144, 512);
    SBAR();
    m5 = __builtin_amdgcn_rcpf(m5); PIN(m5);
    SBAR();
    n0 = FMA(m0, 0.5f, 1.f); PIN(n0);
    SBAR();
    z1 = acc0[5] * rl; PIN(z1);
    SBAR();
    acc1 = MFMA(wf1, xf[13], acc1);
    SBAR();
    wf4 = trfrag(trA + 8192, 512);
    SBAR();
    z5 = FMA(-z5, m5, z5); PIN(z5);
    SBAR();
    m0 = FMA(m0, n0, 1.f); PIN(m0);
    SBAR();
    m1 = __builtin_amdgcn_exp2f(z1); PIN(m1);
    SBAR();
    GL(gw1, kw0 + 640, voffK1, 24576);
    SBAR();
    acc1 = MFMA(wf2, xf[14], acc1);
    SBAR();
    wf5 = trfrag(trA + 10240, 512);
    SBAR();
    hw1 = cvtpk(z4, z5); PIN(hw1);
    SBAR();
    m0 = __builtin_amdgcn_rcpf(m0); PIN(m0);
    SBAR();
    n1 = FMA(m1, 0.5f, 1.f); PIN(n1);
    SBAR();
    z2 = acc0[6] * rl; PIN(z2);
    SBAR();
    acc1 = MFMA(wf3, xf[15], acc1);
    SBAR();
    wf6 = trfrag(trA + 12288, 512);
    SBAR();
    z0 = FMA(-z0, m0, z0); PIN(z0);
    SBAR();
    m1 = FMA(m1, n1, 1.f); PIN(m1);
    SBAR();
    m2 = __builtin_amdgcn_exp2f(z2); PIN(m2);
    SBAR();
    acc1 = MFMA(wf4, xf[16], acc1);
    SBAR();
    wf7 = trfrag(trA + 14336, 512);
    SBAR();
    ALD4(cor, voffC, gco + 3072);
    SBAR();
    m1 = __builtin_amdgcn_rcpf(m1); PIN(m1);
    SBAR();
    n2 = FMA(m2, 0.5f, 1.f); PIN(n2);
    SBAR();
    z3 = acc0[7] * rl; PIN(z3);
    SBAR();
    acc1 = MFMA(wf5, xf[17], acc1);
    SBAR();
    wf8 = trfrag(trA + 16384, 512);
    SBAR();
    z1 = FMA(-z1, m1, z1); PIN(z1);
    SBAR();
    m2 = FMA(m2, n2, 1.f); PIN(m2);
    SBAR();
    m3 = __builtin_amdgcn_exp2f(z3); PIN(m3);
    SBAR();
    GL(gw1, kw0 + 590464, voffK1, 32768);
    SBAR();
    acc1 = MFMA(wf6, xf[18], acc1);
    SBAR();
    wf9 = trfrag(trA + 18432, 512);
    SBAR();
    PF2(2);
    SBAR();
    hw2 = cvtpk(z0, z1); PIN(hw2);
    SBAR();
    m2 = __builtin_amdgcn_rcpf(m2); PIN(m2);
    SBAR();
    n3 = FMA(m3, 0.5f, 1.f); PIN(n3);
    SBAR();
    z4 = acc0[8] * rl; PIN(z4);
    SBAR();
    acc1 = MFMA(wf7, xf[19], acc1);
    SBAR();
    wf10 = trfrag(trA + 20480, 512);
    SBAR();
    z2 = FMA(-z2, m2, z2); PIN(z2);
    SBAR();
    m3 = FMA(m3, n3, 1.f); PIN(m3);
    SBAR();
    m4 = __builtin_amdgcn_exp2f(z4); PIN(m4);
    SBAR();
    acc1 = MFMA(wf8, xf[20], acc1);
    SBAR();
    wf11 = trfrag(trA + 22528, 512);
    SBAR();
    PF2(3);
    SBAR();
    m3 = __builtin_amdgcn_rcpf(m3); PIN(m3);
    SBAR();
    n4 = FMA(m4, 0.5f, 1.f); PIN(n4);
    SBAR();
    z5 = acc0[9] * rl; PIN(z5);
    SBAR();
    acc1 = MFMA(wf9, xf[21], acc1);
    SBAR();
    wf0 = trfrag(trA + 24576, 512);
    SBAR();
    z3 = FMA(-z3, m3, z3); PIN(z3);
    SBAR();
    m4 = FMA(m4, n4, 1.f); PIN(m4);
    SBAR();
    m5 = __builtin_amdgcn_exp2f(z5); PIN(m5);
    SBAR();
    GL(gw1, kw0 + 1180288, voffK1, 40960);
    SBAR();
    acc1 = MFMA(wf10, xf[22], acc1);
    SBAR();
    wf1 = trfrag(trA + 26624, 512);
    wf2 = trfrag(trA + 28672, 512);
    SBAR();
    hw3 = cvtpk(z2, z3); PIN(hw3); ho[4] = __builtin_bit_cast(bf16x8, (u32x4){hw0, hw1, hw2, hw3}); PIN(ho[4]);
    SBAR();
    m4 = __builtin_amdgcn_rcpf(m4); PIN(m4);
    SBAR();
    n5 = FMA(m5, 0.5f, 1.f); PIN(n5);
    SBAR();
    z0 = acc0[10] * rl; PIN(z0);
    SBAR();
    acc1 = MFMA(wf11, xf[23], acc1);
    SBAR();
    asm volatile("s_waitcnt vmcnt(14)\n\ts_barrier" ::: "memory"); SBAR();
    acc1 = MFMA(wf0, xf[24], acc1);
    SBAR();
    wf3 = trfrag(trA + 30720, 512);
    SBAR();
    z4 = FMA(-z4, m4, z4); PIN(z4);
    SBAR();
    m5 = FMA(m5, n5, 1.f); PIN(m5);
    SBAR();
    m0 = __builtin_amdgcn_exp2f(z0); PIN(m0);
    SBAR();
    sth(ho[4], 4096);
    SBAR();
    acc1 = MFMA(wf1, xf[25], acc1);
    SBAR();
    wf4 = trfrag(trA + 32768, 512);
    SBAR();
    m5 = __builtin_amdgcn_rcpf(m5); PIN(m5);
    SBAR();
    n0 = FMA(m0, 0.5f, 1.f); PIN(n0);
    SBAR();
    z1 = acc0[11] * rl; PIN(z1);
    SBAR();
    GL(gw1, kw1 + 640, voffK1, 49152);
    SBAR();
    acc1 = MFMA(wf2, xf[26], acc1);
    SBAR();
    wf5 = trfrag(trA + 34816, 512);
    SBAR();
    z5 = FMA(-z5, m5, z5); PIN(z5);
    SBAR();
    m0 = FMA(m0, n0, 1.f); PIN(m0);
    SBAR();
    m1 = __builtin_amdgcn_exp2f(z1); PIN(m1);
    SBAR();
    acc1 = MFMA(wf3, xf[27], acc1);
    SBAR();
    wf6 = trfrag(trA + 36864, 512);
    SBAR();
    hw0 = cvtpk(z4, z5); PIN(hw0);
    SBAR();
    m0 = __builtin_amdgcn_rcpf(m0); PIN(m0);
    SBAR();
    n1 = FMA(m1, 0.5f, 1.f); PIN(n1);
    SBAR();
    z2 = acc0[12] * rl; PIN(z2);
    SBAR();
    acc1 = MFMA(wf4, xf[28], acc1);
    SBAR();
    wf7 = trfrag(trA + 38912, 512);
    SBAR();
    z0 = FMA(-z0, m0, z0); PIN(z0);
    SBAR();
    m1 = FMA(m1, n1, 1.f); PIN(m1);
    SBAR();
    m2 = __builtin_amdgcn_exp2f(z2); PIN(m2);
    SBAR();
    acc1 = MFMA(wf5, xf[29], acc1);
    SBAR();
    wf8 = trfrag(trA + 40960, 512);
    SBAR();
    VWAIT1(6, cor); CHK_COR(cor, 3);
    SBAR();
    m1 = __builtin_amdgcn_rcpf(m1); PIN(m1);
    SBAR();
    n2 = FMA(m2, 0.5f, 1.f); PIN(n2);
    SBAR();
    z3 = acc0[13] * rl; PIN(z3);
    SBAR();
    GL(gw1, kw1 + 590464, voffK1, 57344);
    SBAR();
    acc1 = MFMA(wf6, xf[30], acc1);
    SBAR();
    wf9 = trfrag(trA + 43008, 512);
    SBAR();
    z1 = FMA(-z1, m1, z1); PIN(z1);
    SBAR();
    m2 = FMA(m2, n2, 1.f); PIN(m2);
    SBAR();
    m3 = __builtin_amdgcn_exp2f(z3); PIN(m3);
    SBAR();
    acc1 = MFMA(wf7, xf[31], acc1);
    SBAR();
    wf10 = trfrag(trA + 45056, 512);
    SBAR();
    hw1 = cvtpk(z0, z1); PIN(hw1);
    SBAR();
    m2 = __builtin_amdgcn_rcpf(m2); PIN(m2);
    SBAR();
    n3 = FMA(m3, 0.5f, 1.f); PIN(n3);
    SBAR();
    z4 = acc0[14] * rl; PIN(z4);
    SBAR();
    acc1 = MFMA(CORF, lnf, acc1);
    SBAR();
    acc1 = MFMA(wf8, xf[32], acc1);
    SBAR();
    wf11 = trfrag(trA + 47104, 512);
    SBAR();
    z2 = FMA(-z2, m2, z2); PIN(z2);
    SBAR();
    m3 = FMA(m3, n3, 1.f); PIN(m3);
    SBAR();
    m4 = __builtin_amdgcn_exp2f(z4); PIN(m4);
    SBAR();
    acc1 = MFMA(wf9, xf[33], acc1);
    SBAR();
    trA += 49152; PIN(trA); wf0 = trfrag(trA + 0, 512);
    SBAR();
    m3 = __builtin_amdgcn_rcpf(m3); PIN(m3);
    SBAR();
    n4 = FMA(m4, 0.5f, 1.f); PIN(n4);
    SBAR();
    z5 = acc0[15] * rl; PIN(z5);
    SBAR();
    GL(gw1, kw1 + 1180288, voffK1, 65536);
    SBAR();
    acc1 = MFMA(wf10, xf[34], acc1);
    SBAR();
    wf1 = trfrag(trA + 2048, 512);
    wf2 = trfrag(trA + 4096, 512);
    SBAR();
    z3 = FMA(-z3, m3, z3); PIN(z3);
    SBAR();
    m4 = FMA(m4, n4, 1.f); PIN(m4);
    SBAR();
    m5 = __builtin_amdgcn_exp2f(z5); PIN(m5);
    SBAR();
    acc1 = MFMA(wf11, xf[35], acc1);
    SBAR();
    asm volatile("s_waitcnt vmcnt(14)\n\ts_barrier" ::: "memory"); SBAR();
    acc0 = MFMA(wf0, xf[0], zero16);
    SBAR();
    wf3 = trfrag(trA + 6144, 512);
    SBAR();
    hw2 = cvtpk(z2, z3); PIN(hw2);
    SBAR();
    m4 = __builtin_amdgcn_rcpf(m4); PIN(m4);
    SBAR();
    n5 = FMA(m5, 0.5f, 1.f); PIN(n5);
    SBAR();
    acc0 = MFMA(wf1, xf[1], acc0);
    SBAR();
    wf4 = trfrag(trA + 8192, 512);
    SBAR();
    z4 = FMA(-z4, m4, z4); PIN(z4);
    SBAR();
    m5 = FMA(m5, n5, 1.f); PIN(m5);
    SBAR();
    z0 = acc1[0] * rl; PIN(z0);
    SBAR();
    GL(gw1, kw2 + 640, voffK1, 73728);
    SBAR();
    acc0 = MFMA(wf2, xf[2], acc0);
    SBAR();
    wf5 = trfrag(trA + 10240, 512);
    SBAR();
    m5 = __builtin_amdgcn_rcpf(m5); PIN(m5);
    SBAR();
    m0 = __builtin_amdgcn_exp2f(z0); PIN(m0);
    SBAR();
    acc0 = MFMA(wf3, xf[3], acc0);
    SBAR();
    wf6 = trfrag(trA + 12288, 512);
    SBAR();
    z5 = FMA(-z5, m5, z5); PIN(z5);
    SBAR();
    n0 = FMA(m0, 0.5f, 1.f); PIN(n0);
    SBAR();
    z1 = acc1[1] * rl; PIN(z1);
    SBAR();
    acc0 = MFMA(wf4, xf[4], acc0);
    SBAR();
    wf7 = trfrag(trA + 14336, 512);
    SBAR();
    hw3 = cvtpk(z4, z5); PIN(hw3); ho[5] = __builtin_bit_cast(bf16x8, (u32x4){hw0, hw1, hw2, hw3}); PIN(ho[5]);
    SBAR();
    m0 = FMA(m0, n0, 1.f); PIN(m0);
    SBAR();
    m1 = __builtin_amdgcn_exp2f(z1); PIN(m1);
    SBAR();
    acc0 = MFMA(wf5, xf[5], acc0);
    SBAR();
    wf8 = trfrag(trA + 16384, 512);
    SBAR();
    m0 = __builtin_amdgcn_rcpf(m0); PIN(m0);
    SBAR();
    n1 = FMA(m1, 0.5f, 1.f); PIN(n1);
    SBAR();
    z2 = acc1[2] * rl; PIN(z2);
    SBAR();
    sth(ho[5], 5120);
    SBAR();
    GL(gw1, kw2 + 590464, voffK1, 81920);
    SBAR();
    acc0 = MFMA(wf6, xf[6], acc0);
    SBAR();
    wf9 = trfrag(trA + 18432, 512);
    SBAR();
    z0 = FMA(-z0, m0, z0); PIN(z0);
    SBAR();
    m1 = FMA(m1, n1, 1.f); PIN(m1);
    SBAR();
    m2 = __builtin_amdgcn_exp2f(z2); PIN(m2);
    SBAR();
    acc0 = MFMA(wf7, xf[7], acc0);
    SBAR();
    wf10 = trfrag(trA + 20480, 512);
    SBAR();
    m1 = __builtin_amdgcn_rcpf(m1); PIN(m1);
    SBAR();
    n2 = FMA(m2, 0.5f, 1.f); PIN(n2);
    SBAR();
    z3 = acc1[3] * rl; PIN(z3);
    SBAR();
    acc0 = MFMA(wf8, xf[8], acc0);
    SBAR();
    wf11 = trfrag(trA + 22528, 512);
    SBAR();
    z1 = FMA(-z1, m1, z1); PIN(z1);
    SBAR();
    m2 = FMA(m2, n2, 1.f); PIN(m2);
    SBAR();
    m3 = __builtin_amdgcn_exp2f(z3); PIN(m3);
    SBAR();
    acc0 = MFMA(wf9, xf[9], acc0);
    SBAR();
    wf0 = trfrag(trA + 24576, 512);
    SBAR();
    hw0 = cvtpk(z0, z1); PIN(hw0);
    SBAR();
    m2 = __builtin_amdgcn_rcpf(m2); PIN(m2);
    SBAR();
    n3 = FMA(m3, 0.5f, 1.f); PIN(n3);
    SBAR();
    z4 = acc1[4] * rl; PIN(z4);
    SBAR();
    GL(gw1, kw2 + 1180288, voffK1, 90112);
    SBAR();
    acc0 = MFMA(wf10, xf[10], acc0);
    SBAR();
    wf1 = trfrag(trA + 26624, 512);
    wf2 = trfrag(trA + 28672, 512);
    SBAR();
    z2 = FMA(-z2, m2, z2); PIN(z2);
    SBAR();
    m3 = FMA(m3, n3, 1.f); PIN(m3);
    SBAR();
    m4 = __builtin_amdgcn_exp2f(z4); PIN(m4);
    SBAR();
    DEF_K2();
    SBAR();
    acc0 = MFMA(wf11, xf[11], acc0);
    SBAR();
    asm volatile("s_waitcnt vmcnt(14)\n\ts_barrier" ::: "memory"); SBAR();
    acc0 = MFMA(wf0, xf[12], acc0);
    SBAR();
    wf3 = trfrag(trA + 30720, 512);
    SBAR();
    m3 = __builtin_amdgcn_rcpf(m3); PIN(m3);
    SBAR();
    n4 = FMA(m4, 0.5f, 1.f); PIN(n4);
    SBAR();
    z5 = acc1[5] * rl; PIN(z5);
    SBAR();
    acc0 = MFMA(wf1, xf[13], acc0);
    SBAR();
    wf4 = trfrag(trA + 32768, 512);
    SBAR();
    z3 = FMA(-z3, m3, z3); PIN(z3);
    SBAR();
    m4 = FMA(m4, n4, 1.f); PIN(m4);
    SBAR();
    m5 = __builtin_amdgcn_exp2f(z5); PIN(m5);
    SBAR();
    GL(gw2, SRC(0) * 442368 + 0 + ROT3(0) * 128, voffK2, 98304);
    SBAR();
    acc0 = MFMA(wf2, xf[14], acc0);
    SBAR();
    wf5 = trfrag(trA + 34816, 512);
    SBAR();
    hw1 = cvtpk(z2, z3); PIN(hw1);
    SBAR();
    m4 = __builtin_amdgcn_rcpf(m4); PIN(m4);
    SBAR();
    n5 = FMA(m5, 0.5f, 1.f); PIN(n5);
    SBAR();
    z0 = acc1[6] * rl; PIN(z0);
    SBAR();
    acc0 = MFMA(wf3, xf[15], acc0);
    SBAR();
    wf6 = trfrag(trA + 36864, 512);
    SBAR();
    z4 = FMA(-z4, m4, z4); PIN(z4);
    SBAR();
    m5 = FMA(m5, n5, 1.f); PIN(m5);
    SBAR();
    m0 = __builtin_amdgcn_exp2f(z0); PIN(m0);
    SBAR();
    acc0 = MFMA(wf4, xf[16], acc0);
    SBAR();
    wf7 = trfrag(trA + 38912, 512);
    SBAR();
    ALD4(cor, voffC, gco + 4096);
    SBAR();
    m5 = __builtin_amdgcn_rcpf(m5); PIN(m5);
    SBAR();
    n0 = FMA(m0, 0.5f, 1.f); PIN(n0);
    SBAR();
    z1 = acc1[7] * rl; PIN(z1);
    SBAR();
    acc0 = MFMA(wf5, xf[17], acc0);
    SBAR();
    wf8 = trfrag(trA + 40960, 512);
    SBAR();
    z5 = FMA(-z5, m5, z5); PIN(z5);
    SBAR();
    m0 = FMA(m0, n0, 1.f); PIN(m0);
    SBAR();
    m1 = __builtin_amdgcn_exp2f(z1); PIN(m1);
    SBAR();
    GL(gw2, SRC(0) * 442368 + 73728 + ROT3(0) * 128, voffK2, 106496);
    SBAR();
    acc0 = MFMA(wf6, xf[18], acc0);
    SBAR();
    wf9 = trfrag(trA + 43008, 512);
    SBAR();
    PFC();
    SBAR();
    hw2 = cvtpk(z4, z5); PIN(hw2);
    SBAR();
    m0 = __builtin_amdgcn_rcpf(m0); PIN(m0);
    SBAR();
    n1 = FMA(m1, 0.5f, 1.f); PIN(n1);
    SBAR();
    z2 = acc1[8] * rl; PIN(z2);
    SBAR();
    acc0 = MFMA(wf7, xf[19], acc0);
    SBAR();
    wf10 = trfrag(trA + 45056, 512);
    SBAR();
    z0 = FMA(-z0, m0, z0); PIN(z0);
    SBAR();
    m1 = FMA(m1, n1, 1.f); PIN(m1);
    SBAR();
    m2 = __builtin_amdgcn_exp2f(z2); PIN(m2);
    SBAR();
    acc0 = MFMA(wf8, xf[20], acc0);
    SBAR();
    wf11 = trfrag(trA + 47104, 512);
    SBAR();
    m1 = __builtin_amdgcn_rcpf(m1); PIN(m1);
    SBAR();
    n2 = FMA(m2, 0.5f, 1.f); PIN(n2);
    SBAR();
    z3 = acc1[9] * rl; PIN(z3);
    SBAR();
    acc0 = MFMA(wf9, xf[21], acc0);
    SBAR();
    trA += -98304; PIN(trA); wf0 = trfrag(trA + 0, 512);
    SBAR();
    z1 = FMA(-z1, m1, z1); PIN(z1);
    SBAR();
    m2 = FMA(m2, n2, 1.f); PIN(m2);
    SBAR();
    m3 = __builtin_amdgcn_exp2f(z3); PIN(m3);
    SBAR();
    GL(gw2, SRC(0) * 442368 + 147456 + ROT3(0) * 128, voffK2, 114688);
    SBAR();
    acc0 = MFMA(wf10, xf[22], acc0);
    SBAR();
    wf1 = trfrag(trA + 2048, 512);
    wf2 = trfrag(trA + 4096, 512);
    SBAR();
    hw3 = cvtpk(z0, z1); PIN(hw3); ho[6] = __builtin_bit_cast(bf16x8, (u32x4){hw0, hw1, hw2, hw3}); PIN(ho[6]);
    SBAR();
    m2 = __builtin_amdgcn_rcpf(m2); PIN(m2);
    SBAR();
    n3 = FMA(m3, 0.5f, 1.f); PIN(n3);
    SBAR();
    z4 = acc1[10] * rl; PIN(z4);
    SBAR();
    acc0 = MFMA(wf11, xf[23], acc0);
    SBAR();
    asm volatile("s_waitcnt vmcnt(7)\n\ts_barrier" ::: "memory"); SBAR();
    acc0 = MFMA(wf0, xf[24], acc0);
    SBAR();
    wf3 = trfrag(trA + 6144, 512);
    SBAR();
    PROG(3u);
    SBAR();
    z2 = FMA(-z2, m2, z2); PIN(z2);
    SBAR();
    m3 = FMA(m3, n3, 1.f); PIN(m3);
    SBAR();
    m4 = __builtin_amdgcn_exp2f(z4); PIN(m4);
    SBAR();
    sth(ho[6], 6144);
    SBAR();
    acc0 = MFMA(wf1, xf[25], acc0);
    SBAR();
    wf4 = trfrag(trA + 8192, 512);
    SBAR();
    m3 = __builtin_amdgcn_rcpf(m3); PIN(m3);
    SBAR();
    n4 = FMA(m4, 0.5f, 1.f); PIN(n4);
    SBAR();
    z5 = acc1[11] * rl; PIN(z5);
    SBAR();
    GL(gw2, SRC(0) * 442368 + 0 + ROT3(1) * 128, voffK2, 122880);
    SBAR();
    acc0 = MFMA(wf2, xf[26], acc0);
    SBAR();
    wf5 = trfrag(trA + 10240, 512);
    SBAR();
    z3 = FMA(-z3, m3, z3); PIN(z3);
    SBAR();
    m4 = FMA(m4, n4, 1.f); PIN(m4);
    SBAR();
    m5 = __builtin_amdgcn_exp2f(z5); PIN(m5);
    SBAR();
    acc0 = MFMA(wf3, xf[27], acc0);
    SBAR();
    wf6 = trfrag(trA + 12288, 512);
    SBAR();
    hw0 = cvtpk(z2, z3); PIN(hw0);
    SBAR();
    m4 = __builtin_amdgcn_rcpf(m4); PIN(m4);
    SBAR();
    n5 = FMA(m5, 0.5f, 1.f); PIN(n5);
    SBAR();
    z0 = acc1[12] * rl; PIN(z0);
    SBAR();
    acc0 = MFMA(wf4, xf[28], acc0);
    SBAR();
    wf7 = trfrag(trA + 14336, 512);
    SBAR();
    z4 = FMA(-z4, m4, z4); PIN(z4);
    SBAR();
    m5 = FMA(m5, n5, 1.f); PIN(m5);
    SBAR();
    m0 = __builtin_amdgcn_exp2f(z0); PIN(m0);
    SBAR();
    acc0 = MFMA(wf5, xf[29], acc0);
    SBAR();
    wf8 = trfrag(trA + 16384, 512);
    SBAR();
    VWAIT1(5, cor); CHK_COR(cor, 4);
    SBAR();
    m5 = __builtin_amdgcn_rcpf(m5); PIN(m5);
    SBAR();
    n0 = FMA(m0, 0.5f, 1.f); PIN(n0);
    SBAR();
    z1 = acc1[13] * rl; PIN(z1);
    SBAR();
    GL(gw2, SRC(0) * 442368 + 73728 + ROT3(1) * 128, voffK2, 131072);
    SBAR();
    acc0 = MFMA(wf6, xf[30], acc0);
    SBAR();
    wf9 = trfrag(trA + 18432, 512);
    SBAR();
    z5 = FMA(-z5, m5, z5); PIN(z5);
    SBAR();
    m0 = FMA(m0, n0, 1.f); PIN(m0);
    SBAR();
    m1 = __builtin_amdgcn_exp2f(z1); PIN(m1);
    SBAR();
    acc0 = MFMA(wf7, xf[31], acc0);
    SBAR();
    wf10 = trfrag(trA + 20480, 512);
    SBAR();
    hw1 = cvtpk(z4, z5); PIN(hw1);
    SBAR();
    m0 = __builtin_amdgcn_rcpf(m0); PIN(m0);
    SBAR();
    n1 = FMA(m1, 0.5f, 1.f); PIN(n1);
    SBAR();
    z2 = acc1[14] * rl; PIN(z2);
    SBAR();
    acc0 = MFMA(CORF, lnf, acc0);
    SBAR();
    acc0 = MFMA(wf8, xf[32], acc0);
    SBAR();
    wf11 = trfrag(trA + 22528, 512);
    SBAR();
    z0 = FMA(-z0, m0, z0); PIN(z0);
    SBAR();
    m1 = FMA(m1, n1, 1.f); PIN(m1);
    SBAR();
    m2 = __builtin_amdgcn_exp2f(z2); PIN(m2);
    SBAR();
    acc0 = MFMA(wf9, xf[33], acc0);
    SBAR();
    wf0 = trfrag(trA + 24576, 512);
    SBAR();
    m1 = __builtin_amdgcn_rcpf(m1); PIN(m1);
    SBAR();
    n2 = FMA(m2, 0.5f, 1.f); PIN(n2);
    SBAR();
    z3 = acc1[15] * rl; PIN(z3);
    SBAR();
    GL(gw2, SRC(0) * 442368 + 147456 + ROT3(1) * 128, voffK2, 139264);
    SBAR();
    acc0 = MFMA(wf10, xf[34], acc0);
    SBAR();
    wf1 = trfrag(trA + 26624, 512);
    wf2 = trfrag(trA + 28672, 512);
    SBAR();
    z1 = FMA(-z1, m1, z1); PIN(z1);
    SBAR();
    m2 = FMA(m2, n2, 1.f); PIN(m2);
    SBAR();
    m3 = __builtin_amdgcn_exp2f(z3); PIN(m3);
    SBAR();
    acc0 = MFMA(wf11, xf[35], acc0);
    SBAR();
    asm volatile("s_waitcnt vmcnt(13)\n\ts_barrier" ::: "memory"); SBAR();
    acc1 = MFMA(wf0, xf[0], zero16);
    SBAR();
    wf3 = trfrag(trA + 30720, 512);
    SBAR();
    hw2 = cvtpk(z0, z1); PIN(hw2);
    SBAR();
    m2 = __builtin_amdgcn_rcpf(m2); PIN(m2);
    SBAR();
    n3 = FMA(m3, 0.5f, 1.f); PIN(n3);
    SBAR();
    acc1 = MFMA(wf1, xf[1], acc1);
    SBAR();
    wf4 = trfrag(trA + 32768, 512);
    SBAR();
    z2 = FMA(-z2, m2, z2); PIN(z2);
    SBAR();
    m3 = FMA(m3, n3, 1.f); PIN(m3);
    SBAR();
    z4 = acc0[0] * rl; PIN(z4);
    SBAR();
    GL(gw2, SRC(0) * 442368 + 0 + ROT3(2) * 128, voffK2, 0);
    SBAR();
    acc1 = MFMA(wf2, xf[2], acc1);
    SBAR();
    wf5 = trfrag(trA + 34816, 512);
    SBAR();
    m3 = __builtin_amdgcn_rcpf(m3); PIN(m3);
    SBAR();
    m4 = __builtin_amdgcn_exp2f(z4); PIN(m4);
    SBAR();
    acc1 = MFMA(wf3, xf[3], acc1);
    SBAR();
    wf6 = trfrag(trA + 36864, 512);
    SBAR();
    z3 = FMA(-z3, m3, z3); PIN(z3);
    SBAR();
    n4 = FMA(m4, 0.5f, 1.f); PIN(n4);
    SBAR();
    z5 = acc0[1] * rl; PIN(z5);
    SBAR();
    acc1 = MFMA(wf4, xf[4], acc1);
    SBAR();
    wf7 = trfrag(trA + 38912, 512);
    SBAR();
    hw3 = cvtpk(z2, z3); PIN(hw3); ho[7] = __builtin_bit_cast(bf16x8, (u32x4){hw0, hw1, hw2, hw3}); PIN(ho[7]);
    SBAR();
    m4 = FMA(m4, n4, 1.f); PIN(m4);
    SBAR();
    m5 = __builtin_amdgcn_exp2f(z5); PIN(m5);
    SBAR();
    acc1 = MFMA(wf5, xf[5], acc1);
    SBAR();
    wf8 = trfrag(trA + 40960, 512);
    SBAR();
    m4 = __builtin_amdgcn_rcpf(m4); PIN(m4);
    SBAR();
    n5 = FMA(m5, 0.5f, 1.f); PIN(n5);
    SBAR();
    z0 = acc0[2] * rl; PIN(z0);
    SBAR();
    sth(ho[7], 7168);
    SBAR();
    GL(gw2, SRC(0) * 442368 + 73728 + ROT3(2) * 128, voffK2, 8192);
    SBAR();
    acc1 = MFMA(wf6, xf[6], acc1);
    SBAR();
    wf9 = trfrag(trA + 43008, 512);
    SBAR();
    z4 = FMA(-z4, m4, z4); PIN(z4);
    SBAR();
    m5 = FMA(m5, n5, 1.f); PIN(m5);
    SBAR();
    m0 = __builtin_amdgcn_exp2f(z0); PIN(m0);
    SBAR();
    acc1 = MFMA(wf7, xf[7], acc1);
    SBAR();
    wf10 = trfrag(trA + 45056, 512);
    SBAR();
    m5 = __builtin_amdgcn_rcpf(m5); PIN(m5);
    SBAR();
    n0 = FMA(m0, 0.5f, 1.f); PIN(n0);
    SBAR();
    z1 = acc0[3] * rl; PIN(z1);
    SBAR();
    acc1 = MFMA(wf8, xf[8], acc1);
    SBAR();
    wf11 = trfrag(trA + 47104, 512);
    SBAR();
    z5 = FMA(-z5, m5, z5); PIN(z5);
    SBAR();
    m0 = FMA(m0, n0, 1.f); PIN(m0);
    SBAR();
    m1 = __builtin_amdgcn_exp2f(z1); PIN(m1);
    SBAR();
    acc1 = MFMA(wf9, xf[9], acc1);
    SBAR();
    trA += 49152; PIN(trA); wf0 = trfrag(trA + 0, 512);
    SBAR();
    hw0 = cvtpk(z4, z5); PIN(hw0);
    SBAR();
    m0 = __builtin_amdgcn_rcpf(m0); PIN(m0);
    SBAR();
    n1 = FMA(m1, 0.5f, 1.f); PIN(n1);
    SBAR();
    z2 = acc0[4] * rl; PIN(z2);
    SBAR();
    GL(gw2, SRC(0) * 442368 + 147456 + ROT3(2) * 128, voffK2, 16384);
    SBAR();
    acc1 = MFMA(wf10, xf[10], acc1);
    SBAR();
    wf1 = trfrag(trA + 2048, 512);
    wf2 = trfrag(trA + 4096, 512);
    SBAR();
    z0 = FMA(-z0, m0, z0); PIN(z0);
    SBAR();
    m1 = FMA(m1, n1, 1.f); PIN(m1);
    SBAR();
    m2 = __builtin_amdgcn_exp2f(z2); PIN(m2);
    SBAR();
    acc1 = MFMA(wf11, xf[11], acc1);
    SBAR();
    asm volatile("s_waitcnt vmcnt(13)\n\ts_barrier" ::: "memory"); SBAR();
    acc1 = MFMA(wf0, xf[12], acc1);
    SBAR();
    wf3 = trfrag(trA + 6144, 512);
    SBAR();
    m1 = __builtin_amdgcn_rcpf(m1); PIN(m1);
    SBAR();
    n2 = FMA(m2, 0.5f, 1.f); PIN(n2);
    SBAR();
    z3 = acc0[5] * rl; PIN(z3);
    SBAR();
    acc1 = MFMA(wf1, xf[13], acc1);
    SBAR();
    wf4 = trfrag(trA + 8192, 512);
    SBAR();
    z1 = FMA(-z1, m1, z1); PIN(z1);
    SBAR();
    m2 = FMA(m2, n2, 1.f); PIN(m2);
    SBAR();
    m3 = __builtin_amdgcn_exp2f(z3); PIN(m3);
    SBAR();
    GL(gw2, SRC(0) * 442368 + 221184 + ROT3(0) * 128, voffK2, 24576);
    SBAR();
    acc1 = MFMA(wf2, xf[14], acc1);
    SBAR();
    wf5 = trfrag(trA + 10240, 512);
    SBAR();
    hw1 = cvtpk(z0, z1); PIN(hw1);
    SBAR();
    m2 = __builtin_amdgcn_rcpf(m2); PIN(m2);
    SBAR();
    n3 = FMA(m3, 0.5f, 1.f); PIN(n3);
    SBAR();
    z4 = acc0[6] * rl; PIN(z4);
    SBAR();
    ALD2(cv, voffG, gdn);
    SBAR();
    acc1 = MFMA(wf3, xf[15], acc1);
    SBAR();
    wf6 = trfrag(trA + 12288, 512);
    SBAR();
    z2 = FMA(-z2, m2, z2); PIN(z2);
    SBAR();
    m3 = FMA(m3, n3, 1.f); PIN(m3);
    SBAR();
    m4 = __builtin_amdgcn_exp2f(z4); PIN(m4);
    SBAR();
    acc1 = MFMA(wf4, xf[16], acc1);
    SBAR();
    wf7 = trfrag(trA + 14336, 512);
    SBAR();
    ALD4(cor, voffC, gco + 5120);
    SBAR();
    m3 = __builtin_amdgcn_rcpf(m3); PIN(m3);
    SBAR();
    n4 = FMA(m4, 0.5f, 1.f); PIN(n4);
    SBAR();
    z5 = acc0[7] * rl; PIN(z5);
    SBAR();
    acc1 = MFMA(wf5, xf[17], acc1);
    SBAR();
    wf8 = trfrag(trA + 16384, 512);
    SBAR();
    z3 = FMA(-z3, m3, z3); PIN(z3);
    SBAR();
    m4 = FMA(m4, n4, 1.f); PIN(m4);
    SBAR();
    m5 = __builtin_amdgcn_exp2f(z5); PIN(m5);
    SBAR();
    GL(gw2, SRC(0) * 442368 + 294912 + ROT3(0) * 128, voffK2, 32768);
    SBAR();
    acc1 = MFMA(wf6, xf[18], acc1);
    SBAR();
    wf9 = trfrag(trA + 18432, 512);
    SBAR();
    hw2 = cvtpk(z2, z3); PIN(hw2);
    SBAR();
    m4 = __builtin_amdgcn_rcpf(m4); PIN(m4);
    SBAR();
    n5 = FMA(m5, 0.5f, 1.f); PIN(n5);
    SBAR();
    z0 = acc0[8] * rl; PIN(z0);
    SBAR();
    acc1 = MFMA(wf7, xf[19], acc1);
    SBAR();
    wf10 = trfrag(trA + 20480, 512);
    SBAR();
    z4 = FMA(-z4, m4, z4); PIN(z4);
    SBAR();
    m5 = FMA(m5, n5, 1.f); PIN(m5);
    SBAR();
    m0 = __builtin_amdgcn_exp2f(z0); PIN(m0);
    SBAR();
    acc1 = MFMA(wf8, xf[20], acc1);
    SBAR();
    wf11 = trfrag(trA + 22528, 512);
    SBAR();
    m5 = __builtin_amdgcn_rcpf(m5); PIN(m5);
    SBAR();
    n0 = FMA(m0, 0.5f, 1.f); PIN(n0);
    SBAR();
    z1 = acc0[9] * rl; PIN(z1);
    SBAR();
    acc1 = MFMA(wf9, xf[21], acc1);
    SBAR();
    wf0 = trfrag(trA + 24576, 512);
    SBAR();
    z5 = FMA(-z5, m5, z5); PIN(z5);
    SBAR();
    m0 = FMA(m0, n0, 1.f); PIN(m0);
    SBAR();
    m1 = __builtin_amdgcn_exp2f(z1); PIN(m1);
    SBAR();
    GL(gw2, SRC(0) * 442368 + 368640 + ROT3(0) * 128, voffK2, 40960);
    SBAR();
    acc1 = MFMA(wf10, xf[22], acc1);
    SBAR();
    wf1 = trfrag(trA + 26624, 512);
    wf2 = trfrag(trA + 28672, 512);
    SBAR();
    hw3 = cvtpk(z4, z5); PIN(hw3); ho[8] = __builtin_bit_cast(bf16x8, (u32x4){hw0, hw1, hw2, hw3}); PIN(ho[8]);
    SBAR();
    m0 = __builtin_amdgcn_rcpf(m0); PIN(m0);
    SBAR();
    n1 = FMA(m1, 0.5f, 1.f); PIN(n1);
    SBAR();
    z2 = acc0[10] * rl; PIN(z2);
    SBAR();
    DEF_TRB();
    SBAR();
    acc1 = MFMA(wf11, xf[23], acc1);
    SBAR();
    asm volatile("s_waitcnt vmcnt(13)\n\ts_barrier" ::: "memory"); SBAR();
    acc1 = MFMA(wf0, xf[24], acc1);
    SBAR();
    wf3 = trfrag(trA + 30720, 512);
    SBAR();
    z0 = FMA(-z0, m0, z0); PIN(z0);
    SBAR();
    m1 = FMA(m1, n1, 1.f); PIN(m1);
    SBAR();
    m2 = __builtin_amdgcn_exp2f(z2); PIN(m2);
    SBAR();
    sth(ho[8], 8192);
    SBAR();
    acc1 = MFMA(wf1, xf[25], acc1);
    SBAR();
    wf4 = trfrag(trA + 32768, 512);
    SBAR();
    m1 = __builtin_amdgcn_rcpf(m1); PIN(m1);
    SBAR();
    n2 = FMA(m2, 0.5f, 1.f); PIN(n2);
    SBAR();
    z3 = acc0[11] * rl; PIN(z3);
    SBAR();
    GL(gw2, SRC(0) * 442368 + 221184 + ROT3(1) * 128, voffK2, 49152);
    SBAR();
    acc1 = MFMA(wf2, xf[26], acc1);
    SBAR();
    wf5 = trfrag(trA + 34816, 512);
    SBAR();
    z1 = FMA(-z1, m1, z1); PIN(z1);
    SBAR();
    m2 = FMA(m2, n2, 1.f); PIN(m2);
    SBAR();
    m3 = __builtin_amdgcn_exp2f(z3); PIN(m3);
    SBAR();
    acc1 = MFMA(wf3, xf[27], acc1);
    SBAR();
    wf6 = trfrag(trA + 36864, 512);
    SBAR();
    hw0 = cvtpk(z0, z1); PIN(hw0);
    SBAR();
    m2 = __builtin_amdgcn_rcpf(m2); PIN(m2);
    SBAR();
    n3 = FMA(m3, 0.5f, 1.f); PIN(n3);
    SBAR();
    z4 = acc0[12] * rl; PIN(z4);
    SBAR();
    acc1 = MFMA(wf4, xf[28], acc1);
    SBAR();
    wf7 = trfrag(trA + 38912, 512);
    SBAR();
    z2 = FMA(-z2, m2, z2); PIN(z2);
    SBAR();
    m3 = FMA(m3, n3, 1.f); PIN(m3);
    SBAR();
    m4 = __builtin_amdgcn_exp2f(z4); PIN(m4);
    SBAR();
    acc1 = MFMA(wf5, xf[29], acc1);
    SBAR();
    wf8 = trfrag(trA + 40960, 512);
    SBAR();
    VWAIT1(4, cor); CHK_COR(cor, 5);
    SBAR();
    m3 = __builtin_amdgcn_rcpf(m3); PIN(m3);
    SBAR();
    n4 = FMA(m4, 0.5f, 1.f); PIN(n4);
    SBAR();
    z5 = acc0[13] * rl; PIN(z5);
    SBAR();
    GL(gw2, SRC(0) * 442368 + 294912 + ROT3(1) * 128, voffK2, 57344);
    SBAR();
    acc1 = MFMA(wf6, xf[30], acc1);
    SBAR();
    wf9 = trfrag(trA + 43008, 512);
    SBAR();
    z3 = FMA(-z3, m3, z3); PIN(z3);
    SBAR();
    m4 = FMA(m4, n4, 1.f); PIN(m4);
    SBAR();
    m5 = __builtin_amdgcn_exp2f(z5); PIN(m5);
    SBAR();
    acc1 = MFMA(wf7, xf[31], acc1);
    SBAR();
    wf10 = trfrag(trA + 45056, 512);
    SBAR();
    hw1 = cvtpk(z2, z3); PIN(hw1);
    SBAR();
    m4 = __builtin_amdgcn_rcpf(m4); PIN(m4);
    SBAR();
    n5 = FMA(m5, 0.5f, 1.f); PIN(n5);
    SBAR();
    z0 = acc0[14] * rl; PIN(z0);
    SBAR();
    acc1 = MFMA(CORF, lnf, acc1);
    SBAR();
    acc1 = MFMA(wf8, xf[32], acc1);
    SBAR();
    wf11 = trfrag(trA + 47104, 512);
    SBAR();
    z4 = FMA(-z4, m4, z4); PIN(z4);
    SBAR();
    m5 = FMA(m5, n5, 1.f); PIN(m5);
    SBAR();
    m0 = __builtin_amdgcn_exp2f(z0); PIN(m0);
    SBAR();
    acc1 = MFMA(wf9, xf[33], acc1);
    SBAR();
    trB0 += 98304; PIN(trB0); wf0 = trfrag(trB0 + 0, 1024);
    trB1 += 98304; PIN(trB1); wf1 = trfrag(trB1 + 0, 1024);
    SBAR();
    m5 = __builtin_amdgcn_rcpf(m5); PIN(m5);
    SBAR();
    n0 = FMA(m0, 0.5f, 1.f); PIN(n0);
    SBAR();
    z1 = acc0[15] * rl; PIN(z1);
    SBAR();
    GL(gw2, SRC(0) * 442368 + 368640 + ROT3(1) * 128, voffK2, 65536);
    SBAR();
    acc1 = MFMA(wf10, xf[34], acc1);
    SBAR();
    wf2 = trfrag(trB0 + 2048, 1024);
    wf3 = trfrag(trB1 + 2048, 1024);
    SBAR();
    z5 = FMA(-z5, m5, z5); PIN(z5);
    SBAR();
    m0 = FMA(m0, n0, 1.f); PIN(m0);
    SBAR();
    m1 = __builtin_amdgcn_exp2f(z1); PIN(m1);
    SBAR();
    acc1 = MFMA(wf11, xf[35], acc1);
    SBAR();
    asm volatile("s_waitcnt vmcnt(13)\n\ts_barrier" ::: "memory"); SBAR();
    bcc[0] = MFMA(wf0, ho[0], zero16);
    SBAR();
    wf4 = trfrag(trB0 + 8192, 1024);
    SBAR();
    hw2 = cvtpk(z4, z5); PIN(hw2);
    SBAR();
    m0 = __builtin_amdgcn_rcpf(m0); PIN(m0);
    SBAR();
    n1 = FMA(m1, 0.5f, 1.f); PIN(n1);
    SBAR();
    bcc[1] = MFMA(wf1, ho[0], zero16);
    SBAR();
    wf5 = trfrag(trB1 + 8192, 1024);
    SBAR();
    z0 = FMA(-z0, m0, z0); PIN(z0);
    SBAR();
    m1 = FMA(m1, n1, 1.f); PIN(m1);
    SBAR();
    z2 = acc1[0] * rl; PIN(z2);
    SBAR();
    GL(gw2, SRC(0) * 442368 + 221184 + ROT3(2) * 128, voffK2, 73728);
    SBAR();
    bcc[0] = MFMA(wf2, ho[1], bcc[0]);
    SBAR();
    wf6 = trfrag(trB0 + 10240, 1024);
    SBAR();
    m1 = __builtin_amdgcn_rcpf(m1); PIN(m1);
    SBAR();
    m2 = __builtin_amdgcn_exp2f(z2); PIN(m2);
    SBAR();
    z3 = acc1[1] * rl; PIN(z3);
    SBAR();
    VWAIT1(8, cv); CHK_H(3u);
    SBAR();
    bcc[1] = MFMA(wf3, ho[1], bcc[1]);
    SBAR();
    wf7 = trfrag(trB1 + 10240, 1024);
    SBAR();
    z1 = FMA(-z1, m1, z1); PIN(z1);
    SBAR();
    n2 = FMA(m2, 0.5f, 1.f); PIN(n2);
    SBAR();
    m3 = __builtin_amdgcn_exp2f(z3); PIN(m3);
    SBAR();
    ALD4B(hp[0], voffS, gh1 + 0);
    SBAR();
    bcc[0] = MFMA(wf4, ho[2], bcc[0]);
    SBAR();
    wf8 = trfrag(trB0 + 16384, 1024);
    SBAR();
    hw3 = cvtpk(z0, z1); PIN(hw3); ho[9] = __builtin_bit_cast(bf16x8, (u32x4){hw0, hw1, hw2, hw3}); PIN(ho[9]);
    SBAR();
    m2 = FMA(m2, n2, 1.f); PIN(m2);
    SBAR();
    n3 = FMA(m3, 0.5f, 1.f); PIN(n3);
    SBAR();
    z4 = acc1[2] * rl; PIN(z4);
    SBAR();
    bcc[1] = MFMA(wf5, ho[2], bcc[1]);
    SBAR();
    wf9 = trfrag(trB1 + 16384, 1024);
    SBAR();
    m2 = __builtin_amdgcn_rcpf(m2); PIN(m2);
    SBAR();
    m3 = FMA(m3, n3, 1.f); PIN(m3);
    SBAR();
    m4 = __builtin_amdgcn_exp2f(z4); PIN(m4);
    SBAR();
    sth(ho[9], 9216);
    SBAR();
    GL(gw2, SRC(0) * 442368 + 294912 + ROT3(2) * 128, voffK2, 81920);
    SBAR();
    bcc[0] = MFMA(wf6, ho[3], bcc[0]);
    SBAR();
    wf10 = trfrag(trB0 + 18432, 1024);
    SBAR();
    z2 = FMA(-z2, m2, z2); PIN(z2);
    SBAR();
    m3 = __builtin_amdgcn_rcpf(m3); PIN(m3);
    SBAR();
    n4 = FMA(m4, 0.5f, 1.f); PIN(n4);
    SBAR();
    z5 = acc1[3] * rl; PIN(z5);
    SBAR();
    bcc[1] = MFMA(wf7, ho[3], bcc[1]);
    SBAR();
    wf11 = trfrag(trB1 + 18432, 1024);
    SBAR();
    z3 = FMA(-z3, m3, z3); PIN(z3);
    SBAR();
    m4 = FMA(m4, n4, 1.f); PIN(m4);
    SBAR();
    m5 = __builtin_amdgcn_exp2f(z5); PIN(m5);
    SBAR();
    z0 = acc1[4] * rl; PIN(z0);
    SBAR();
    ALD4B(hp[1], voffS, gh1 + 1024);
    SBAR();
    bcc[0] = MFMA(wf8, ho[4], bcc[0]);
    SBAR();
    wf0 = trfrag(trB0 + 24576, 1024);
    SBAR();
    hw0 = cvtpk(z2, z3); PIN(hw0);
    SBAR();
    m4 = __builtin_amdgcn_rcpf(m4); PIN(m4);
    SBAR();
    n5 = FMA(m5, 0.5f, 1.f); PIN(n5);
    SBAR();
    m0 = __builtin_amdgcn_exp2f(z0); PIN(m0);
    SBAR();
    bcc[1] = MFMA(wf9, ho[4], bcc[1]);
    SBAR();
    wf1 = trfrag(trB1 + 24576, 1024);
    SBAR();
    z4 = FMA(-z4, m4, z4); PIN(z4);
    SBAR();
    m5 = FMA(m5, n5, 1.f); PIN(m5);
    SBAR();
    n0 = FMA(m0, 0.5f, 1.f); PIN(n0);
    SBAR();
    z1 = acc1[5] * rl; PIN(z1);
    SBAR();
    GL(gw2, SRC(0) * 442368 + 368640 + ROT3(2) * 128, voffK2, 90112);
    SBAR();
    bcc[0] = MFMA(wf10, ho[5], bcc[0]);
    SBAR();
    wf2 = trfrag(trB0 + 26624, 1024);
    wf3 = trfrag(trB1 + 26624, 1024);
    SBAR();
    m5 = __builtin_amdgcn_rcpf(m5); PIN(m5);
    SBAR();
    m0 = FMA(m0, n0, 1.f); PIN(m0);
    SBAR();
    m1 = __builtin_amdgcn_exp2f(z1); PIN(m1);
    SBAR();
    bcc[1] = MFMA(wf11, ho[5], bcc[1]);
    SBAR();
    asm volatile("s_waitcnt vmcnt(15)\n\ts_barrier" ::: "memory"); SBAR();
    bcc[2] = MFMA(wf0, ho[0], zero16);
    SBAR();
    wf4 = trfrag(trB0 + 32768, 1024);
    SBAR();
    z5 = FMA(-z5, m5, z5); PIN(z5);
    SBAR();
    m0 = __builtin_amdgcn_rcpf(m0); PIN(m0);
    SBAR();
    n1 = FMA(m1, 0.5f, 1.f); PIN(n1);
    SBAR();
    z2 = acc1[6] * rl; PIN(z2);
    SBAR();
    bcc[3] = MFMA(wf1, ho[0], zero16);
    SBAR();
    wf5 = trfrag(trB1 + 32768, 1024);
    SBAR();
    hw1 = cvtpk(z4, z5); PIN(hw1);
    SBAR();
    z0 = FMA(-z0, m0, z0); PIN(z0);
    SBAR();
    m1 = FMA(m1, n1, 1.f); PIN(m1);
    SBAR();
    m2 = __builtin_amdgcn_exp2f(z2); PIN(m2);
    SBAR();
    GL(gw2, SRC(1) * 442368 + 0 + ROT3(0) * 128, voffK2, 98304);
    SBAR();
    bcc[2] = MFMA(wf2, ho[1], bcc[2]);
    SBAR();
    wf6 = trfrag(trB0 + 34816, 1024);
    SBAR();
    m1 = __builtin_amdgcn_rcpf(m1); PIN(m1);
    SBAR();
    n2 = FMA(m2, 0.5f, 1.f); PIN(n2);
    SBAR();
    z3 = acc1[7] * rl; PIN(z3);
    SBAR();
    bcc[3] = MFMA(wf3, ho[1], bcc[3]);
    SBAR();
    wf7 = trfrag(trB1 + 34816, 1024);
    SBAR();
    z1 = FMA(-z1, m1, z1); PIN(z1);
    SBAR();
    m2 = FMA(m2, n2, 1.f); PIN(m2);
    SBAR();
    m3 = __builtin_amdgcn_exp2f(z3); PIN(m3);
    SBAR();
    z4 = acc1[8] * rl; PIN(z4);
    SBAR();
    ALD4B(hp[2], voffS, gh1 + 2048);
    SBAR();
    bcc[2] = MFMA(wf4, ho[2], bcc[2]);
    SBAR();
    wf8 = trfrag(trB0 + 40960, 1024);
    SBAR();
    hw2 = cvtpk(z0, z1); PIN(hw2);
    SBAR();
    m2 = __builtin_amdgcn_rcpf(m2); PIN(m2);
    SBAR();
    n3 = FMA(m3, 0.5f, 1.f); PIN(n3);
    SBAR();
    m4 = __builtin_amdgcn_exp2f(z4); PIN(m4);
    SBAR();
    bcc[3] = MFMA(wf5, ho[2], bcc[3]);
    SBAR();
    wf9 = trfrag(trB1 + 40960, 1024);
    SBAR();
    z2 = FMA(-z2, m2, z2); PIN(z2);
    SBAR();
    m3 = FMA(m3, n3, 1.f); PIN(m3);
    SBAR();
    n4 = FMA(m4, 0.5f, 1.f); PIN(n4);
    SBAR();
    z5 = acc1[9] * rl; PIN(z5);
    SBAR();
    GL(gw2, SRC(1) * 442368 + 73728 + ROT3(0) * 128, voffK2, 106496);
    SBAR();
    bcc[2] = MFMA(wf6, ho[3], bcc[2]);
    SBAR();
    wf10 = trfrag(trB0 + 43008, 1024);
    SBAR();
    m3 = __builtin_amdgcn_rcpf(m3); PIN(m3);
    SBAR();
    m4 = FMA(m4, n4, 1.f); PIN(m4);
    SBAR();
    m5 = __builtin_amdgcn_exp2f(z5); PIN(m5);
    SBAR();
    bcc[3] = MFMA(wf7, ho[3], bcc[3]);
    SBAR();
    wf11 = trfrag(trB1 + 43008, 1024);
    SBAR();
    z3 = FMA(-z3, m3, z3); PIN(z3);
    SBAR();
    m4 = __builtin_amdgcn_rcpf(m4); PIN(m4);
    SBAR();
    n5 = FMA(m5, 0.5f, 1.f); PIN(n5);
    SBAR();
    z0 = acc1[10] * rl; PIN(z0);
    SBAR();
    ALD4B(hp[3], voffS, gh1 + 3072);
    SBAR();
    bcc[2] = MFMA(wf8, ho[4], bcc[2]);
    SBAR();
    trB0 += -98304; PIN(trB0); wf0 = trfrag(trB0 + 0, 1024);
    SBAR();
    hw3 = cvtpk(z2, z3); PIN(hw3); ho[10] = __builtin_bit_cast(bf16x8, (u32x4){hw0, hw1, hw2, hw3}); PIN(ho[10]);
    SBAR();
    z4 = FMA(-z4, m4, z4); PIN(z4);
    SBAR();
    m5 = FMA(m5, n5, 1.f); PIN(m5);
    SBAR();
    m0 = __builtin_amdgcn_exp2f(z0); PIN(m0);
    SBAR();
    bcc[3] = MFMA(wf9, ho[4], bcc[3]);
    SBAR();
    trB1 += -98304; PIN(trB1); wf1 = trfrag(trB1 + 0, 1024);
    SBAR();
    m5 = __builtin_amdgcn_rcpf(m5); PIN(m5);
    SBAR();
    n0 = FMA(m0, 0.5f, 1.f); PIN(n0);
    SBAR();
    z1 = acc1[11] * rl; PIN(z1);
    SBAR();
    sth(ho[10], 10240);
    SBAR();
    GL(gw2, SRC(1) * 442368 + 147456 + ROT3(0) * 128, voffK2, 114688);
    SBAR();
    bcc[2] = MFMA(wf10, ho[5], bcc[2]);
    SBAR();
    wf2 = trfrag(trB0 + 2048, 1024);
    wf3 = trfrag(trB1 + 2048, 1024);
    SBAR();
    z5 = FMA(-z5, m5, z5); PIN(z5);
    SBAR();
    m0 = FMA(m0, n0, 1.f); PIN(m0);
    SBAR();
    m1 = __builtin_amdgcn_exp2f(z1); PIN(m1);
    SBAR();
    z2 = acc1[12] * rl; PIN(z2);
    SBAR();
    bcc[3] = MFMA(wf11, ho[5], bcc[3]);
    SBAR();
    asm volatile("s_waitcnt vmcnt(16)\n\ts_barrier" ::: "memory"); SBAR();
    bcc[4] = MFMA(wf0, ho[0], zero16);
    SBAR();
    wf4 = trfrag(trB0 + 8192, 1024);
    SBAR();
    hw0 = cvtpk(z4, z5); PIN(hw0);
    SBAR();
    m0 = __builtin_amdgcn_rcpf(m0); PIN(m0);
    SBAR();
    n1 = FMA(m1, 0.5f, 1.f); PIN(n1);
    SBAR();
    m2 = __builtin_amdgcn_exp2f(z2); PIN(m2);
    SBAR();
    bcc[5] = MFMA(wf1, ho[0], zero16);
    SBAR();
    wf5 = trfrag(trB1 + 8192, 1024);
    SBAR();
    z0 = FMA(-z0, m0, z0); PIN(z0);
    SBAR();
    m1 = FMA(m1, n1, 1.f); PIN(m1);
    SBAR();
    n2 = FMA(m2, 0.5f, 1.f); PIN(n2);
    SBAR();
    z3 = acc1[13] * rl; PIN(z3);
    SBAR();
    GL(gw2, SRC(1) * 442368 + 0 + ROT3(1) * 128, voffK2, 122880);
    SBAR();
    bcc[4] = MFMA(wf2, ho[1], bcc[4]);
    SBAR();
    wf6 = trfrag(trB0 + 10240, 1024);
    SBAR();
    m1 = __builtin_amdgcn_rcpf(m1); PIN(m1);
    SBAR();
    m2 = FMA(m2, n2, 1.f); PIN(m2);
    SBAR();
    m3 = __builtin_amdgcn_exp2f(z3); PIN(m3);
    SBAR();
    bcc[5] = MFMA(wf3, ho[1], bcc[5]);
    SBAR();
    wf7 = trfrag(trB1 + 10240, 1024);
    SBAR();
    z1 = FMA(-z1, m1, z1); PIN(z1);
    SBAR();
    m2 = __builtin_amdgcn_rcpf(m2); PIN(m2);
    SBAR();
    n3 = FMA(m3, 0.5f, 1.f); PIN(n3);
    SBAR();
    z4 = acc1[14] * rl; PIN(z4);
    SBAR();
    ALD4B(hp[4], voffS, gh1 + 4096);
    SBAR();
    bcc[4] = MFMA(wf4, ho[2], bcc[4]);
    SBAR();
    wf8 = trfrag(trB0 + 16384, 1024);
    SBAR();
    hw1 = cvtpk(z0, z1); PIN(hw1);
    SBAR();
    z2 = FMA(-z2, m2, z2); PIN(z2);
    SBAR();
    m3 = FMA(m3, n3, 1.f); PIN(m3);
    SBAR();
    m4 = __builtin_amdgcn_exp2f(z4); PIN(m4);
    SBAR();
    bcc[5] = MFMA(wf5, ho[2], bcc[5]);
    SBAR();
    wf9 = trfrag(trB1 + 16384, 1024);
    SBAR();
    m3 = __builtin_amdgcn_rcpf(m3); PIN(m3);
    SBAR();
    n4 = FMA(m4, 0.5f, 1.f); PIN(n4);
    SBAR();
    z5 = acc1[15] * rl; PIN(z5);
    SBAR();
    GL(gw2, SRC(1) * 442368 + 73728 + ROT3(1) * 128, voffK2, 131072);
    SBAR();
    bcc[4] = MFMA(wf6, ho[3], bcc[4]);
    SBAR();
    wf10 = trfrag(trB0 + 18432, 1024);
    SBAR();
    z3 = FMA(-z3, m3, z3); PIN(z3);
    SBAR();
    m4 = FMA(m4, n4, 1.f); PIN(m4);
    SBAR();
    m5 = __builtin_amdgcn_exp2f(z5); PIN(m5);
    SBAR();
    bcc[5] = MFMA(wf7, ho[3], bcc[5]);
    SBAR();
    wf11 = trfrag(trB1 + 18432, 1024);
    SBAR();
    hw2 = cvtpk(z2, z3); PIN(hw2);
    SBAR();
    m4 = __builtin_amdgcn_rcpf(m4); PIN(m4);
    SBAR();
    n5 = FMA(m5, 0.5f, 1.f); PIN(n5);
    SBAR();
    ALD4B(hp[5], voffS, gh1 + 5120);
    SBAR();
    bcc[4] = MFMA(wf8, ho[4], bcc[4]);
    SBAR();
    wf0 = trfrag(trB0 + 24576, 1024);
    SBAR();
    z4 = FMA(-z4, m4, z4); PIN(z4);
    SBAR();
    m5 = FMA(m5, n5, 1.f); PIN(m5);
    SBAR();
    bcc[5] = MFMA(wf9, ho[4], bcc[5]);
    SBAR();
    wf1 = trfrag(trB1 + 24576, 1024);
    SBAR();
    m5 = __builtin_amdgcn_rcpf(m5); PIN(m5);
    SBAR();
    GL(gw2, SRC(1) * 442368 + 147456 + ROT3(1) * 128, voffK2, 139264);
    SBAR();
    bcc[4] = MFMA(wf10, ho[5], bcc[4]);
    SBAR();
    wf2 = trfrag(trB0 + 26624, 1024);
    wf3 = trfrag(trB1 + 26624, 1024);
    SBAR();
    z5 = FMA(-z5, m5, z5); PIN(z5);
    SBAR();
    bcc[5] = MFMA(wf11, ho[5], bcc[5]);
    SBAR();
    asm volatile("s_waitcnt vmcnt(17)\n\ts_barrier" ::: "memory"); SBAR();
    bcc[0] = MFMA(wf0, ho[6], bcc[0]);
    SBAR();
    wf4 = trfrag(trB0 + 32768, 1024);
    SBAR();
    hw3 = cvtpk(z4, z5); PIN(hw3); ho[11] = __builtin_bit_cast(bf16x8, (u32x4){hw0, hw1, hw2, hw3}); PIN(ho[11]);
    SBAR();
    bcc[1] = MFMA(wf1, ho[6], bcc[1]);
    SBAR();
    wf5 = trfrag(trB1 + 32768, 1024);
    SBAR();
    sth(ho[11], 11264);
    SBAR();
    GL(gw2, SRC(1) * 442368 + 0 + ROT3(2) * 128, voffK2, 0);
    SBAR();
    bcc[0] = MFMA(wf2, ho[7], bcc[0]);
    SBAR();
    wf6 = trfrag(trB0 + 34816, 1024);
    SBAR();
    bcc[1] = MFMA(wf3, ho[7], bcc[1]);
    SBAR();
    wf7 = trfrag(trB1 + 34816, 1024);
    SBAR();
    ALD4B(ho[0], voffS, gh2 + 0);
    SBAR();
    bcc[0] = MFMA(wf4, ho[8], bcc[0]);
    SBAR();
    wf8 = trfrag(trB0 + 40960, 1024);
    SBAR();
    bcc[1] = MFMA(wf5, ho[8], bcc[1]);
    SBAR();
    wf9 = trfrag(trB1 + 40960, 1024);
    SBAR();
    GL(gw2, SRC(1) * 442368 + 73728 + ROT3(2) * 128, voffK2, 8192);
    SBAR();
    bcc[0] = MFMA(wf6, ho[9], bcc[0]);
    SBAR();
    wf10 = trfrag(trB0 + 43008, 1024);
    SBAR();
    bcc[1] = MFMA(wf7, ho[9], bcc[1]);
    SBAR();
    wf11 = trfrag(trB1 + 43008, 1024);
    SBAR();
    ALD4B(ho[1], voffS, gh2 + 1024);
    SBAR();
    bcc[0] = MFMA(wf8, ho[10], bcc[0]);
    SBAR();
    trB0 += 49152; PIN(trB0); wf0 = trfrag(trB0 + 0, 1024);
    SBAR();
    bcc[1] = MFMA(wf9, ho[10], bcc[1]);
    SBAR();
    trB1 += 49152; PIN(trB1); wf1 = trfrag(trB1 + 0, 1024);
    SBAR();
    GL(gw2, SRC(1) * 442368 + 147456 + ROT3(2) * 128, voffK2, 16384);
    SBAR();
    bcc[0] = MFMA(wf10, ho[11], bcc[0]);
    SBAR();
    wf2 = trfrag(trB0 + 2048, 1024);
    wf3 = trfrag(trB1 + 2048, 1024);
    SBAR();
    bcc[1] = MFMA(wf11, ho[11], bcc[1]);
    SBAR();
    asm volatile("s_waitcnt vmcnt(17)\n\ts_barrier" ::: "memory"); SBAR();
    bcc[2] = MFMA(wf0, ho[6], bcc[2]);
    SBAR();
    wf4 = trfrag(trB0 + 8192, 1024);
    SBAR();
    bcc[3] = MFMA(wf1, ho[6], bcc[3]);
    SBAR();
    wf5 = trfrag(trB1 + 8192, 1024);
    SBAR();
    GL(gw2, SRC(2) * 442368 + 0 + ROT3(0) * 128, voffK2, 24576);
    SBAR();
    bcc[2] = MFMA(wf2, ho[7], bcc[2]);
    SBAR();
    wf6 = trfrag(trB0 + 10240, 1024);
    SBAR();
    bcc[3] = MFMA(wf3, ho[7], bcc[3]);
    SBAR();
    wf7 = trfrag(trB1 + 10240, 1024);
    SBAR();
    ALD4B(ho[2], voffS, gh2 + 2048);
    SBAR();
    bcc[2] = MFMA(wf4, ho[8], bcc[2]);
    SBAR();
    wf8 = trfrag(trB0 + 16384, 1024);
    SBAR();
    bcc[3] = MFMA(wf5, ho[8], bcc[3]);
    SBAR();
    wf9 = trfrag(trB1 + 16384, 1024);
    SBAR();
    GL(gw2, SRC(2) * 442368 + 73728 + ROT3(0) * 128, voffK2, 32768);
    SBAR();
    bcc[2] = MFMA(wf6, ho[9], bcc[2]);
    SBAR();
    wf10 = trfrag(trB0 + 18432, 1024);
    SBAR();
    bcc[3] = MFMA(wf7, ho[9], bcc[3]);
    SBAR();
    wf11 = trfrag(trB1 + 18432, 1024);
    SBAR();
    ALD4B(ho[3], voffS, gh2 + 3072);
    SBAR();
    bcc[2] = MFMA(wf8, ho[10], bcc[2]);
    SBAR();
    wf0 = trfrag(trB0 + 24576, 1024);
    SBAR();
    bcc[3] = MFMA(wf9, ho[10], bcc[3]);
    SBAR();
    wf1 = trfrag(trB1 + 24576, 1024);
    SBAR();
    GL(gw2, SRC(2) * 442368 + 147456 + ROT3(0) * 128, voffK2, 40960);
    SBAR();
    bcc[2] = MFMA(wf10, ho[11], bcc[2]);
    SBAR();
    wf2 = trfrag(trB0 + 26624, 1024);
    wf3 = trfrag(trB1 + 26624, 1024);
    SBAR();
    bcc[3] = MFMA(wf11, ho[11], bcc[3]);
    SBAR();
    asm volatile("s_waitcnt vmcnt(10)\n\ts_barrier" ::: "memory"); SBAR();
    bcc[4] = MFMA(wf0, ho[6], bcc[4]);
    SBAR();
    wf4 = trfrag(trB0 + 32768, 1024);
    SBAR();
    PROG(6u);
    SBAR();
    bcc[5] = MFMA(wf1, ho[6], bcc[5]);
    SBAR();
    wf5 = trfrag(trB1 + 32768, 1024);
    SBAR();
    GL(gw2, SRC(2) * 442368 + 0 + ROT3(1) * 128, voffK2, 49152);
    SBAR();
    bcc[4] = MFMA(wf2, ho[7], bcc[4]);
    SBAR();
    wf6 = trfrag(trB0 + 34816, 1024);
    SBAR();
    bcc[5] = MFMA(wf3, ho[7], bcc[5]);
    SBAR();
    wf7 = trfrag(trB1 + 34816, 1024);
    SBAR();
    ALD4B(ho[4], voffS, gh2 + 4096);
    SBAR();
    bcc[4] = MFMA(wf4, ho[8], bcc[4]);
    SBAR();
    wf8 = trfrag(trB0 + 40960, 1024);
    SBAR();
    bcc[5] = MFMA(wf5, ho[8], bcc[5]);
    SBAR();
    wf9 = trfrag(trB1 + 40960, 1024);
    SBAR();
    GL(gw2, SRC(2) * 442368 + 73728 + ROT3(1) * 128, voffK2, 57344);
    SBAR();
    bcc[4] = MFMA(wf6, ho[9], bcc[4]);
    SBAR();
    wf10 = trfrag(trB0 + 43008, 1024);
    SBAR();
    bcc[5] = MFMA(wf7, ho[9], bcc[5]);
    SBAR();
    wf11 = trfrag(trB1 + 43008, 1024);
    SBAR();
    ALD4B(ho[5], voffS, gh2 + 5120);
    SBAR();
    bcc[4] = MFMA(wf8, ho[10], bcc[4]);
    SBAR();
    trB0 += 49152; PIN(trB0); wf0 = trfrag(trB0 + 0, 1024);
    SBAR();
    bcc[5] = MFMA(wf9, ho[10], bcc[5]);
    SBAR();
    trB1 += 49152; PIN(trB1); wf1 = trfrag(trB1 + 0, 1024);
    SBAR();
    GL(gw2, SRC(2) * 442368 + 147456 + ROT3(1) * 128, voffK2, 65536);
    SBAR();
    bcc[4] = MFMA(wf10, ho[11], bcc[4]);
    SBAR();
    wf2 = trfrag(trB0 + 2048, 1024);
    wf3 = trfrag(trB1 + 2048, 1024);
    SBAR();
    bcc[5] = MFMA(wf11, ho[11], bcc[5]);
    SBAR();
    asm volatile("s_waitcnt vmcnt(16)\n\ts_barrier" ::: "memory"); SBAR();
    PINH(hp[0], hp[1], hp[2], hp[3], hp[4], hp[5]);
    bcc[0] = MFMA(wf0, hp[0], bcc[0]);
    SBAR();
    wf4 = trfrag(trB0 + 8192, 1024);
    SBAR();
    bcc[1] = MFMA(wf1, hp[0], bcc[1]);
    SBAR();
    wf5 = trfrag(trB1 + 8192, 1024);
    SBAR();
    GL(gw2, SRC(2) * 442368 + 0 + ROT3(2) * 128, voffK2, 73728);
    SBAR();
    bcc[0] = MFMA(wf2, hp[1], bcc[0]);
    SBAR();
    wf6 = trfrag(trB0 + 10240, 1024);
    SBAR();
    bcc[1] = MFMA(wf3, hp[1], bcc[1]);
    SBAR();
    wf7 = trfrag(trB1 + 10240, 1024);
    SBAR();
    bcc[0] = MFMA(wf4, hp[2], bcc[0]);
    SBAR();
    wf8 = trfrag(trB0 + 16384, 1024);
    SBAR();
    bcc[1] = MFMA(wf5, hp[2], bcc[1]);
    SBAR();
    wf9 = trfrag(trB1 + 16384, 1024);
    SBAR();
    GL(gw2, SRC(2) * 442368 + 73728 + ROT3(2) * 128, voffK2, 81920);
    SBAR();
    bcc[0] = MFMA(wf6, hp[3], bcc[0]);
    SBAR();
    wf10 = trfrag(trB0 + 18432, 1024);
    SBAR();
    bcc[1] = MFMA(wf7, hp[3], bcc[1]);
    SBAR();
    wf11 = trfrag(trB1 + 18432, 1024);
    SBAR();
    bcc[0] = MFMA(wf8, hp[4], bcc[0]);
    SBAR();
    wf0 = trfrag(trB0 + 24576, 1024);
    SBAR();
    bcc[1] = MFMA(wf9, hp[4], bcc[1]);
    SBAR();
    wf1 = trfrag(trB1 + 24576, 1024);
    SBAR();
    GL(gw2, SRC(2) * 442368 + 147456 + ROT3(2) * 128, voffK2, 90112);
    SBAR();
    bcc[0] = MFMA(wf10, hp[5], bcc[0]);
    SBAR();
    wf2 = trfrag(trB0 + 26624, 1024);
    wf3 = trfrag(trB1 + 26624, 1024);
    SBAR();
    bcc[1] = MFMA(wf11, hp[5], bcc[1]);
    SBAR();
    asm volatile("s_waitcnt vmcnt(13)\n\ts_barrier" ::: "memory"); SBAR();
    bcc[2] = MFMA(wf0, hp[0], bcc[2]);
    SBAR();
    wf4 = trfrag(trB0 + 32768, 1024);
    SBAR();
    ALD2(cv, voffG, gdn);
    SBAR();
    bcc[3] = MFMA(wf1, hp[0], bcc[3]);
    SBAR();
    wf5 = trfrag(trB1 + 32768, 1024);
    SBAR();
    GL(gw2, SRC(1) * 442368 + 221184 + ROT3(0) * 128, voffK2, 98304);
    SBAR();
    bcc[2] = MFMA(wf2, hp[1], bcc[2]);
    SBAR();
    wf6 = trfrag(trB0 + 34816, 1024);
    SBAR();
    bcc[3] = MFMA(wf3, hp[1], bcc[3]);
    SBAR();
    wf7 = trfrag(trB1 + 34816, 1024);
    SBAR();
    bcc[2] = MFMA(wf4, hp[2], bcc[2]);
    SBAR();
    wf8 = trfrag(trB0 + 40960, 1024);
    SBAR();
    bcc[3] = MFMA(wf5, hp[2], bcc[3]);
    SBAR();
    wf9 = trfrag(trB1 + 40960, 1024);
    SBAR();
    GL(gw2, SRC(1) * 442368 + 294912 + ROT3(0) * 128, voffK2, 106496);
    SBAR();
    bcc[2] = MFMA(wf6, hp[3], bcc[2]);
    SBAR();
    wf10 = trfrag(trB0 + 43008, 1024);
    SBAR();
    bcc[3] = MFMA(wf7, hp[3], bcc[3]);
    SBAR();
    wf11 = trfrag(trB1 + 43008, 1024);
    SBAR();
    bcc[2] = MFMA(wf8, hp[4], bcc[2]);
    SBAR();
    trB0 += -98304; PIN(trB0); wf0 = trfrag(trB0 + 0, 1024);
    SBAR();
    bcc[3] = MFMA(wf9, hp[4], bcc[3]);
    SBAR();
    trB1 += -98304; PIN(trB1); wf1 = trfrag(trB1 + 0, 1024);
    SBAR();
    GL(gw2, SRC(1) * 442368 + 368640 + ROT3(0) * 128, voffK2, 114688);
    SBAR();
    bcc[2] = MFMA(wf10, hp[5], bcc[2]);
    SBAR();
    wf2 = trfrag(trB0 + 2048, 1024);
    wf3 = trfrag(trB1 + 2048, 1024);
    SBAR();
    bcc[3] = MFMA(wf11, hp[5], bcc[3]);
    SBAR();
    asm volatile("s_waitcnt vmcnt(12)\n\ts_barrier" ::: "memory"); SBAR();
    bcc[4] = MFMA(wf0, hp[0], bcc[4]);
    SBAR();
    wf4 = trfrag(trB0 + 8192, 1024);
    SBAR();
    bcc[5] = MFMA(wf1, hp[0], bcc[5]);
    SBAR();
    wf5 = trfrag(trB1 + 8192, 1024);
    SBAR();
    GL(gw2, SRC(2) * 442368 + 221184 + ROT3(0) * 128, voffK2, 122880);
    SBAR();
    bcc[4] = MFMA(wf2, hp[1], bcc[4]);
    SBAR();
    wf6 = trfrag(trB0 + 10240, 1024);
    SBAR();
    VWAIT1(4, cv); CHK_H(6u);
    SBAR();
    bcc[5] = MFMA(wf3, hp[1], bcc[5]);
    SBAR();
    wf7 = trfrag(trB1 + 10240, 1024);
    SBAR();
    ALD4B(hp[6], voffS, gh1 + 6144);
    SBAR();
    bcc[4] = MFMA(wf4, hp[2], bcc[4]);
    SBAR();
    wf8 = trfrag(trB0 + 16384, 1024);
    SBAR();
    bcc[5] = MFMA(wf5, hp[2], bcc[5]);
    SBAR();
    wf9 = trfrag(trB1 + 16384, 1024);
    SBAR();
    GL(gw2, SRC(2) * 442368 + 294912 + ROT3(0) * 128, voffK2, 131072);
    SBAR();
    bcc[4] = MFMA(wf6, hp[3], bcc[4]);
    SBAR();
    wf10 = trfrag(trB0 + 18432, 1024);
    SBAR();
    ALD4B(hp[7], voffS, gh1 + 7168);
    SBAR();
    bcc[5] = MFMA(wf7, hp[3], bcc[5]);
    SBAR();
    wf11 = trfrag(trB1 + 18432, 1024);
    SBAR();
    bcc[4] = MFMA(wf8, hp[4], bcc[4]);
    SBAR();
    wf0 = trfrag(trB0 + 24576, 1024);
    SBAR();
    bcc[5] = MFMA(wf9, hp[4], bcc[5]);
    SBAR();
    wf1 = trfrag(trB1 + 24576, 1024);
    SBAR();
    ALD4B(hp[8], voffS, gh1 + 8192);
    SBAR();
    GL(gw2, SRC(2) * 442368 + 368640 + ROT3(0) * 128, voffK2, 139264);
    SBAR();
    bcc[4] = MFMA(wf10, hp[5], bcc[4]);
    SBAR();
    wf2 = trfrag(trB0 + 26624, 1024);
    wf3 = trfrag(trB1 + 26624, 1024);
    SBAR();
    bcc[5] = MFMA(wf11, hp[5], bcc[5]);
    SBAR();
    asm volatile("s_waitcnt vmcnt(13)\n\ts_barrier" ::: "memory"); SBAR();
    PINH(ho[0], ho[1], ho[2], ho[3], ho[4], ho[5]);
    bcc[0] = MFMA(wf0, ho[0], bcc[0]);
    SBAR();
    wf4 = trfrag(trB0 + 32768, 1024);
    SBAR();
    bcc[1] = MFMA(wf1, ho[0], bcc[1]);
    SBAR();
    wf5 = trfrag(trB1 + 32768, 1024);
    SBAR();
    ALD4B(hp[9], voffS, gh1 + 9216);
    SBAR();
    GL(gw2, SRC(1) * 442368 + 221184 + ROT3(1) * 128, voffK2, 0);
    SBAR();
    bcc[0] = MFMA(wf2, ho[1], bcc[0]);
    SBAR();
    wf6 = trfrag(trB0 + 34816, 1024);
    SBAR();
    bcc[1] = MFMA(wf3, ho[1], bcc[1]);
    SBAR();
    wf7 = trfrag(trB1 + 34816, 1024);
    SBAR();
    bcc[0] = MFMA(wf4, ho[2], bcc[0]);
    SBAR();
    wf8 = trfrag(trB0 + 40960, 1024);
    SBAR();
    ALD4B(hp[10], voffS, gh1 + 10240);
    SBAR();
    bcc[1] = MFMA(wf5, ho[2], bcc[1]);
    SBAR();
    wf9 = trfrag(trB1 + 40960, 1024);
    SBAR();
    GL(gw2, SRC(1) * 442368 + 294912 + ROT3(1) * 128, voffK2, 8192);
    SBAR();
    bcc[0] = MFMA(wf6, ho[3], bcc[0]);
    SBAR();
    wf10 = trfrag(trB0 + 43008, 1024);
    SBAR();
    bcc[1] = MFMA(wf7, ho[3], bcc[1]);
    SBAR();
    wf11 = trfrag(trB1 + 43008, 1024);
    SBAR();
    ALD4B(hp[11], voffS, gh1 + 11264);
    SBAR();
    bcc[0] = MFMA(wf8, ho[4], bcc[0]);
    SBAR();
    trB0 += 49152; PIN(trB0); wf0 = trfrag(trB0 + 0, 1024);
    SBAR();
    bcc[1] = MFMA(wf9, ho[4], bcc[1]);
    SBAR();
    trB1 += 49152; PIN(trB1); wf1 = trfrag(trB1 + 0, 1024);
    SBAR();
    GL(gw2, SRC(1) * 442368 + 368640 + ROT3(1) * 128, voffK2, 16384);
    SBAR();
    bcc[0] = MFMA(wf10, ho[5], bcc[0]);
    SBAR();
    wf2 = trfrag(trB0 + 2048, 1024);
    wf3 = trfrag(trB1 + 2048, 1024);
    SBAR();
    ALD4B(ho[6], voffS, gh2 + 6144);
    SBAR();
    bcc[1] = MFMA(wf11, ho[5], bcc[1]);
    SBAR();
    asm volatile("s_waitcnt vmcnt(17)\n\ts_barrier" ::: "memory"); SBAR();
    bcc[2] = MFMA(wf0, ho[0], bcc[2]);
    SBAR();
    wf4 = trfrag(trB0 + 8192, 1024);
    SBAR();
    bcc[3] = MFMA(wf1, ho[0], bcc[3]);
    SBAR();
    wf5 = trfrag(trB1 + 8192, 1024);
    SBAR();
    GL(gw2, SRC(2) * 442368 + 221184 + ROT3(1) * 128, voffK2, 24576);
    SBAR();
    bcc[2] = MFMA(wf2, ho[1], bcc[2]);
    SBAR();
    wf6 = trfrag(trB0 + 10240, 1024);
    SBAR();
    ALD4B(ho[7], voffS, gh2 + 7168);
    SBAR();
    bcc[3] = MFMA(wf3, ho[1], bcc[3]);
    SBAR();
    wf7 = trfrag(trB1 + 10240, 1024);
    SBAR();
    bcc[2] = MFMA(wf4, ho[2], bcc[2]);
    SBAR();
    wf8 = trfrag(trB0 + 16384, 1024);
    SBAR();
    bcc[3] = MFMA(wf5, ho[2], bcc[3]);
    SBAR();
    wf9 = trfrag(trB1 + 16384, 1024);
    SBAR();
    ALD4B(ho[8], voffS, gh2 + 8192);
    SBAR();
    GL(gw2, SRC(2) * 442368 + 294912 + ROT3(1) * 128, voffK2, 32768);
    SBAR();
    bcc[2] = MFMA(wf6, ho[3], bcc[2]);
    SBAR();
    wf10 = trfrag(trB0 + 18432, 1024);
    SBAR();
    bcc[3] = MFMA(wf7, ho[3], bcc[3]);
    SBAR();
    wf11 = trfrag(trB1 + 18432, 1024);
    SBAR();
    bcc[2] = MFMA(wf8, ho[4], bcc[2]);
    SBAR();
    wf0 = trfrag(trB0 + 24576, 1024);
    SBAR();
    ALD4B(ho[9], voffS, gh2 + 9216);
    SBAR();
    bcc[3] = MFMA(wf9, ho[4], bcc[3]);
    SBAR();
    wf1 = trfrag(trB1 + 24576, 1024);
    SBAR();
    GL(gw2, SRC(2) * 442368 + 368640 + ROT3(1) * 128, voffK2, 40960);
    SBAR();
    bcc[2] = MFMA(wf10, ho[5], bcc[2]);
    SBAR();
    wf2 = trfrag(trB0 + 26624, 1024);
    wf3 = trfrag(trB1 + 26624, 1024);
    SBAR();
    bcc[3] = MFMA(wf11, ho[5], bcc[3]);
    SBAR();
    asm volatile("s_waitcnt vmcnt(19)\n\ts_barrier" ::: "memory"); SBAR();
    bcc[4] = MFMA(wf0, ho[0], bcc[4]);
    SBAR();
    wf4 = trfrag(trB0 + 32768, 1024);
    SBAR();
    ALD4B(ho[10], voffS, gh2 + 10240);
    SBAR();
    bcc[5] = MFMA(wf1, ho[0], bcc[5]);
    SBAR();
    wf5 = trfrag(trB1 + 32768, 1024);
    SBAR();
    GL(gw2, SRC(1) * 442368 + 221184 + ROT3(2) * 128, voffK2, 49152);
    SBAR();
    bcc[4] = MFMA(wf2, ho[1], bcc[4]);
    SBAR();
    wf6 = trfrag(trB0 + 34816, 1024);
    SBAR();
    bcc[5] = MFMA(wf3, ho[1], bcc[5]);
    SBAR();
    wf7 = trfrag(trB1 + 34816, 1024);
    SBAR();
    ALD4B(ho[11], voffS, gh2 + 11264);
    SBAR();
    bcc[4] = MFMA(wf4, ho[2], bcc[4]);
    SBAR();
    wf8 = trfrag(trB0 + 40960, 1024);
    SBAR();
    bcc[5] = MFMA(wf5, ho[2], bcc[5]);
    SBAR();
    wf9 = trfrag(trB1 + 40960, 1024);
    SBAR();
    GL(gw2, SRC(1) * 442368 + 294912 + ROT3(2) * 128, voffK2, 57344);
    SBAR();
    bcc[4] = MFMA(wf6, ho[3], bcc[4]);
    SBAR();
    wf10 = trfrag(trB0 + 43008, 1024);
    SBAR();
    bcc[5] = MFMA(wf7, ho[3], bcc[5]);
    SBAR();
    wf11 = trfrag(trB1 + 43008, 1024);
    SBAR();
    bcc[4] = MFMA(wf8, ho[4], bcc[4]);
    SBAR();
    trB0 += 49152; PIN(trB0); wf0 = trfrag(trB0 + 0, 1024);
    SBAR();
    bcc[5] = MFMA(wf9, ho[4], bcc[5]);
    SBAR();
    trB1 += 49152; PIN(trB1); wf1 = trfrag(trB1 + 0, 1024);
    SBAR();
    GL(gw2, SRC(1) * 442368 + 368640 + ROT3(2) * 128, voffK2, 65536);
    SBAR();
    bcc[4] = MFMA(wf10, ho[5], bcc[4]);
    SBAR();
    wf2 = trfrag(trB0 + 2048, 1024);
    wf3 = trfrag(trB1 + 2048, 1024);
    SBAR();
    bcc[5] = MFMA(wf11, ho[5], bcc[5]);
    SBAR();
    asm volatile("s_waitcnt vmcnt(13)\n\ts_barrier" ::: "memory"); SBAR();
    PINH(hp[6], hp[7], hp[8], hp[9], hp[10], hp[11]);
    bcc[0] = MFMA(wf0, hp[6], bcc[0]);
    SBAR();
    wf4 = trfrag(trB0 + 8192, 1024);
    SBAR();
    bcc[1] = MFMA(wf1, hp[6], bcc[1]);
    SBAR();
    wf5 = trfrag(trB1 + 8192, 1024);
    SBAR();
    GL(gw2, SRC(2) * 442368 + 221184 + ROT3(2) * 128, voffK2, 73728);
    SBAR();
    bcc[0] = MFMA(wf2, hp[7], bcc[0]);
    SBAR();
    wf6 = trfrag(trB0 + 10240, 1024);
    SBAR();
    bcc[1] = MFMA(wf3, hp[7], bcc[1]);
    SBAR();
    wf7 = trfrag(trB1 + 10240, 1024);
    SBAR();
    bcc[0] = MFMA(wf4, hp[8], bcc[0]);
    SBAR();
    wf8 = trfrag(trB0 + 16384, 1024);
    SBAR();
    bcc[1] = MFMA(wf5, hp[8], bcc[1]);
    SBAR();
    wf9 = trfrag(trB1 + 16384, 1024);
    SBAR();
    GL(gw2, SRC(2) * 442368 + 294912 + ROT3(2) * 128, voffK2, 81920);
    SBAR();
    bcc[0] = MFMA(wf6, hp[9], bcc[0]);
    SBAR();
    wf10 = trfrag(trB0 + 18432, 1024);
    SBAR();
    bcc[1] = MFMA(wf7, hp[9], bcc[1]);
    SBAR();
    wf11 = trfrag(trB1 + 18432, 1024);
    SBAR();
    bcc[0] = MFMA(wf8, hp[10], bcc[0]);
    SBAR();
    wf0 = trfrag(trB0 + 24576, 1024);
    SBAR();
    bcc[1] = MFMA(wf9, hp[10], bcc[1]);
    SBAR();
    wf1 = trfrag(trB1 + 24576, 1024);
    SBAR();
    GL(gw2, SRC(2) * 442368 + 368640 + ROT3(2) * 128, voffK2, 90112);
    SBAR();
    bcc[0] = MFMA(wf10, hp[11], bcc[0]);
    SBAR();
    wf2 = trfrag(trB0 + 26624, 1024);
    wf3 = trfrag(trB1 + 26624, 1024);
    SBAR();
    bcc[1] = MFMA(wf11, hp[11], bcc[1]);
    SBAR();
    asm volatile("s_waitcnt vmcnt(5)\n\ts_barrier" ::: "memory"); SBAR();
    PINH(ho[6], ho[7], ho[8], ho[9], ho[10], ho[11]);
    bcc[0] = MFMA(wf0, ho[6], bcc[0]);
    SBAR();
    wf4 = trfrag(trB0 + 32768, 1024);
    SBAR();
    bcc[1] = MFMA(wf1, ho[6], bcc[1]);
    SBAR();
    wf5 = trfrag(trB1 + 32768, 1024);
    SBAR();
    bcc[0] = MFMA(wf2, ho[7], bcc[0]);
    SBAR();
    wf6 = trfrag(trB0 + 34816, 1024);
    SBAR();
    DEF_XCH();
    SBAR();
    bcc[1] = MFMA(wf3, ho[7], bcc[1]);
    SBAR();
    wf7 = trfrag(trB1 + 34816, 1024);
    SBAR();
    bcc[0] = MFMA(wf4, ho[8], bcc[0]);
    SBAR();
    wf8 = trfrag(trB0 + 40960, 1024);
    SBAR();
    bcc[1] = MFMA(wf5, ho[8], bcc[1]);
    SBAR();
    wf9 = trfrag(trB1 + 40960, 1024);
    SBAR();
    bcc[0] = MFMA(wf6, ho[9], bcc[0]);
    SBAR();
    wf10 = trfrag(trB0 + 43008, 1024);
    SBAR();
    bcc[1] = MFMA(wf7, ho[9], bcc[1]);
    SBAR();
    wf11 = trfrag(trB1 + 43008, 1024);
    SBAR();
    bcc[0] = MFMA(wf8, ho[10], bcc[0]);
    SBAR();
    trB0 += -98304; PIN(trB0); wf0 = trfrag(trB0 + 0, 1024);
    SBAR();
    bcc[1] = MFMA(wf9, ho[10], bcc[1]);
    SBAR();
    trB1 += -98304; PIN(trB1); wf1 = trfrag(trB1 + 0, 1024);
    SBAR();
    bcc[0] = MFMA(wf10, ho[11], bcc[0]);
    SBAR();
    wf2 = trfrag(trB0 + 2048, 1024);
    wf3 = trfrag(trB1 + 2048, 1024);
    SBAR();
    bcc[1] = MFMA(wf11, ho[11], bcc[1]);
    SBAR();
    asm volatile("s_waitcnt vmcnt(8)\n\ts_barrier" ::: "memory"); SBAR();
    bcc[2] = MFMA(wf0, hp[6], bcc[2]);
    SBAR();
    wf4 = trfrag(trB0 + 8192, 1024);
    SBAR();
    bcc[3] = MFMA(wf1, hp[6], bcc[3]);
    SBAR();
    wf5 = trfrag(trB1 + 8192, 1024);
    SBAR();
    xw += 98304; PIN(xw); *(LAS f32x4*)(xw + 0) = (f32x4){bcc[1][0], bcc[1][1], bcc[1][2], bcc[1][3]};
    SBAR();
    bcc[2] = MFMA(wf2, hp[7], bcc[2]);
    SBAR();
    wf6 = trfrag(trB0 + 10240, 1024);
    SBAR();
    XST(0);
    SBAR();
    bcc[3] = MFMA(wf3, hp[7], bcc[3]);
    SBAR();
    wf7 = trfrag(trB1 + 10240, 1024);
    SBAR();
    *(LAS f32x4*)(xw + 1024) = (f32x4){bcc[1][4], bcc[1][5], bcc[1][6], bcc[1][7]};
    SBAR();
    bcc[2] = MFMA(wf4, hp[8], bcc[2]);
    SBAR();
    wf8 = trfrag(trB0 + 16384, 1024);
    SBAR();
    bcc[3] = MFMA(wf5, hp[8], bcc[3]);
    SBAR();
    wf9 = trfrag(trB1 + 16384, 1024);
    SBAR();
    *(LAS f32x4*)(xw + 2048) = (f32x4){bcc[1][8], bcc[1][9], bcc[1][10], bcc[1][11]};
    SBAR();
    bcc[2] = MFMA(wf6, hp[9], bcc[2]);
    SBAR();
    wf10 = trfrag(trB0 + 18432, 1024);
    SBAR();
    bcc[3] = MFMA(wf7, hp[9], bcc[3]);
    SBAR();
    wf11 = trfrag(trB1 + 18432, 1024);
    SBAR();
    *(LAS f32x4*)(xw + 3072) = (f32x4){bcc[1][12], bcc[1][13], bcc[1][14], bcc[1][15]};
    SBAR();
    bcc[2] = MFMA(wf8, hp[10], bcc[2]);
    SBAR();
    wf0 = trfrag(trB0 + 24576, 1024);
    SBAR();
    XST(1);
    SBAR();
    bcc[3] = MFMA(wf9, hp[10], bcc[3]);
    SBAR();
    wf1 = trfrag(trB1 + 24576, 1024);
    SBAR();
    bcc[2] = MFMA(wf10, hp[11], bcc[2]);
    SBAR();
    wf2 = trfrag(trB0 + 26624, 1024);
    wf3 = trfrag(trB1 + 26624, 1024);
    SBAR();
    bcc[3] = MFMA(wf11, hp[11], bcc[3]);
    SBAR();
    asm volatile("s_waitcnt vmcnt(5) lgkmcnt(4)\n\ts_barrier" ::: "memory"); SBAR();
    bcc[2] = MFMA(wf0, ho[6], bcc[2]);
    SBAR();
    wf4 = trfrag(trB0 + 32768, 1024);
    SBAR();
    xrd += 98304; PIN(xrd); xv0 = ldf4(xrd + 0);
    SBAR();
    bcc[3] = MFMA(wf1, ho[6], bcc[3]);
    SBAR();
    wf5 = trfrag(trB1 + 32768, 1024);
    SBAR();
    xv1 = ldf4(xrd + 1024);
    SBAR();
    bcc[2] = MFMA(wf2, ho[7], bcc[2]);
    SBAR();
    wf6 = trfrag(trB0 + 34816, 1024);
    SBAR();
    { float t_ = bcc[0][0] + xv0[0]; PIN(t_); bcc[0][0] = t_; } SBAR(); { float t_ = bcc[0][1] + xv0[1]; PIN(t_); bcc[0][1] = t_; } SBAR(); { float t_ = bcc[0][2] + xv0[2]; PIN(t_); bcc[0][2] = t_; } SBAR(); { float t_ = bcc[0][3] + xv0[3]; PIN(t_); bcc[0][3] = t_; } SBAR();
    SBAR();
    xv2 = ldf4(xrd + 2048);
    SBAR();
    bcc[3] = MFMA(wf3, ho[7], bcc[3]);
    SBAR();
    wf7 = trfrag(trB1 + 34816, 1024);
    SBAR();
    { float t_ = bcc[0][4] + xv1[0]; PIN(t_); bcc[0][4] = t_; } SBAR(); { float t_ = bcc[0][5] + xv1[1]; PIN(t_); bcc[0][5] = t_; } SBAR(); { float t_ = bcc[0][6] + xv1[2]; PIN(t_); bcc[0][6] = t_; } SBAR(); { float t_ = bcc[0][7] + xv1[3]; PIN(t_); bcc[0][7] = t_; } SBAR();
    SBAR();
    xv3 = ldf4(xrd + 3072);
    SBAR();
    bcc[2] = MFMA(wf4, ho[8], bcc[2]);
    SBAR();
    wf8 = trfrag(trB0 + 40960, 1024);
    SBAR();
    { float t_ = bcc[0][8] + xv2[0]; PIN(t_); bcc[0][8] = t_; } SBAR(); { float t_ = bcc[0][9] + xv2[1]; PIN(t_); bcc[0][9] = t_; } SBAR(); { float t_ = bcc[0][10] + xv2[2]; PIN(t_); bcc[0][10] = t_; } SBAR(); { float t_ = bcc[0][11] + xv2[3]; PIN(t_); bcc[0][11] = t_; } SBAR();
    SBAR();
    pk0[0] = cvtpk(bcc[0][0], bcc[0][1]);
    SBAR();
    pk0[1] = cvtpk(bcc[0][2], bcc[0][3]);
    SBAR();
    bcc[3] = MFMA(wf5, ho[8], bcc[3]);
    SBAR();
    wf9 = trfrag(trB1 + 40960, 1024);
    SBAR();
    { float t_ = bcc[0][12] + xv3[0]; PIN(t_); bcc[0][12] = t_; } SBAR(); { float t_ = bcc[0][13] + xv3[1]; PIN(t_); bcc[0][13] = t_; } SBAR(); { float t_ = bcc[0][14] + xv3[2]; PIN(t_); bcc[0][14] = t_; } SBAR(); { float t_ = bcc[0][15] + xv3[3]; PIN(t_); bcc[0][15] = t_; } SBAR();
    SBAR();
    pk0[2] = cvtpk(bcc[0][4], bcc[0][5]);
    SBAR();
    pk0[3] = cvtpk(bcc[0][6], bcc[0][7]); PIN(pk0);
    SBAR();
    bcc[2] = MFMA(wf6, ho[9], bcc[2]);
    SBAR();
    wf10 = trfrag(trB0 + 43008, 1024);
    SBAR();
    pk1[0] = cvtpk(bcc[0][8], bcc[0][9]);
    SBAR();
    pk1[1] = cvtpk(bcc[0][10], bcc[0][11]);
    SBAR();
    stpart(pk0, ROT3(0) * 114688); stu4(kp, pk0);
    SBAR();
    bcc[3] = MFMA(wf7, ho[9], bcc[3]);
    SBAR();
    wf11 = trfrag(trB1 + 43008, 1024);
    SBAR();
    pk1[2] = cvtpk(bcc[0][12], bcc[0][13]);
    SBAR();
    pk1[3] = cvtpk(bcc[0][14], bcc[0][15]); PIN(pk1);
    SBAR();
    bcc[2] = MFMA(wf8, ho[10], bcc[2]);
    SBAR();
    trB0 += 49152; PIN(trB0); wf0 = trfrag(trB0 + 0, 1024);
    SBAR();
    stpart(pk1, ROT3(0) * 114688 + 1024); stu4(kp + 1024, pk1); kp += kstep; PIN(kp);
    SBAR();
    bcc[3] = MFMA(wf9, ho[10], bcc[3]);
    SBAR();
    trB1 += 49152; PIN(trB1); wf1 = trfrag(trB1 + 0, 1024);
    SBAR();
    bcc[2] = MFMA(wf10, ho[11], bcc[2]);
    SBAR();
    wf2 = trfrag(trB0 + 2048, 1024);
    wf3 = trfrag(trB1 + 2048, 1024);
    SBAR();
    bcc[3] = MFMA(wf11, ho[11], bcc[3]);
    SBAR();
    asm volatile("s_waitcnt vmcnt(4)\n\ts_barrier" ::: "memory"); SBAR();
    bcc[4] = MFMA(wf0, hp[6], bcc[4]);
    SBAR();
    wf4 = trfrag(trB0 + 8192, 1024);
    SBAR();
    bcc[5] = MFMA(wf1, hp[6], bcc[5]);
    SBAR();
    wf5 = trfrag(trB1 + 8192, 1024);
    SBAR();
    xw += -98304; PIN(xw); *(LAS f32x4*)(xw + 0) = (f32x4){bcc[3][0], bcc[3][1], bcc[3][2], bcc[3][3]};
    SBAR();
    bcc[4] = MFMA(wf2, hp[7], bcc[4]);
    SBAR();
    wf6 = trfrag(trB0 + 10240, 1024);
    SBAR();
    bcc[5] = MFMA(wf3, hp[7], bcc[5]);
    SBAR();
    wf7 = trfrag(trB1 + 10240, 1024);
    SBAR();
    *(LAS f32x4*)(xw + 1024) = (f32x4){bcc[3][4], bcc[3][5], bcc[3][6], bcc[3][7]};
    SBAR();
    bcc[4] = MFMA(wf4, hp[8], bcc[4]);
    SBAR();
    wf8 = trfrag(trB0 + 16384, 1024);
    SBAR();
    bcc[5] = MFMA(wf5, hp[8], bcc[5]);
    SBAR();
    wf9 = trfrag(trB1 + 16384, 1024);
    SBAR();
    *(LAS f32x4*)(xw + 2048) = (f32x4){bcc[3][8], bcc[3][9], bcc[3][10], bcc[3][11]};
    SBAR();
    bcc[4] = MFMA(wf6, hp[9], bcc[4]);
    SBAR();
    wf10 = trfrag(trB0 + 18432, 1024);
    SBAR();
    bcc[5] = MFMA(wf7, hp[9], bcc[5]);
    SBAR();
    wf11 = trfrag(trB1 + 18432, 1024);
    SBAR();
    *(LAS f32x4*)(xw + 3072) = (f32x4){bcc[3][12], bcc[3][13], bcc[3][14], bcc[3][15]};
    SBAR();
    bcc[4] = MFMA(wf8, hp[10], bcc[4]);
    SBAR();
    wf0 = trfrag(trB1 + 24576, 1024);
    SBAR();
    bcc[5] = MFMA(wf9, hp[10], bcc[5]);
    SBAR();
    wf1 = trfrag(trB1 + 26624, 1024);
    SBAR();
    bcc[4] = MFMA(wf10, hp[11], bcc[4]);
    SBAR();
    wf2 = trfrag(trB1 + 32768, 1024);
    wf3 = trfrag(trB1 + 34816, 1024);
    SBAR();
    bcc[5] = MFMA(wf11, hp[11], bcc[5]);
    SBAR();
    asm volatile("s_waitcnt vmcnt(0) lgkmcnt(4)\n\ts_barrier" ::: "memory"); SBAR();
    bcc[5] = MFMA(wf0, ho[6], bcc[5]);
    SBAR();
    wf4 = trfrag(trB1 + 40960, 1024);
    SBAR();
    PROG(7u);
    SBAR();
    xrd += -98304; PIN(xrd); xv0 = ldf4(xrd + 0);
    SBAR();
    bcc[5] = MFMA(wf1, ho[7], bcc[5]);
    SBAR();
    wf5 = trfrag(trB1 + 43008, 1024);
    SBAR();
    xv1 = ldf4(xrd + 1024);
    SBAR();
    bcc[5] = MFMA(wf2, ho[8], bcc[5]);
    SBAR();
    wf6 = trfrag(trB0 + 24576, 1024);
    SBAR();
    { float t_ = bcc[2][0] + xv0[0]; PIN(t_); bcc[2][0] = t_; } SBAR(); { float t_ = bcc[2][1] + xv0[1]; PIN(t_); bcc[2][1] = t_; } SBAR(); { float t_ = bcc[2][2] + xv0[2]; PIN(t_); bcc[2][2] = t_; } SBAR(); { float t_ = bcc[2][3] + xv0[3]; PIN(t_); bcc[2][3] = t_; } SBAR();
    SBAR();
    xv2 = ldf4(xrd + 2048);
    SBAR();
    bcc[5] = MFMA(wf3, ho[9], bcc[5]);
    SBAR();
    wf7 = trfrag(trB0 + 26624, 1024);
    SBAR();
    { float t_ = bcc[2][4] + xv1[0]; PIN(t_); bcc[2][4] = t_; } SBAR(); { float t_ = bcc[2][5] + xv1[1]; PIN(t_); bcc[2][5] = t_; } SBAR(); { float t_ = bcc[2][6] + xv1[2]; PIN(t_); bcc[2][6] = t_; } SBAR(); { float t_ = bcc[2][7] + xv1[3]; PIN(t_); bcc[2][7] = t_; } SBAR();
    SBAR();
    xv3 = ldf4(xrd + 3072);
    SBAR();
    bcc[5] = MFMA(wf4, ho[10], bcc[5]);
    SBAR();
    wf8 = trfrag(trB0 + 32768, 1024);
    SBAR();
    { float t_ = bcc[2][8] + xv2[0]; PIN(t_); bcc[2][8] = t_; } SBAR(); { float t_ = bcc[2][9] + xv2[1]; PIN(t_); bcc[2][9] = t_; } SBAR(); { float t_ = bcc[2][10] + xv2[2]; PIN(t_); bcc[2][10] = t_; } SBAR(); { float t_ = bcc[2][11] + xv2[3]; PIN(t_); bcc[2][11] = t_; } SBAR();
    SBAR();
    pk0[0] = cvtpk(bcc[2][0], bcc[2][1]);
    SBAR();
    pk0[1] = cvtpk(bcc[2][2], bcc[2][3]);
    SBAR();
    bcc[5] = MFMA(wf5, ho[11], bcc[5]);
    SBAR();
    wf9 = trfrag(trB0 + 34816, 1024);
    SBAR();
    { float t_ = bcc[2][12] + xv3[0]; PIN(t_); bcc[2][12] = t_; } SBAR(); { float t_ = bcc[2][13] + xv3[1]; PIN(t_); bcc[2][13] = t_; } SBAR(); { float t_ = bcc[2][14] + xv3[2]; PIN(t_); bcc[2][14] = t_; } SBAR(); { float t_ = bcc[2][15] + xv3[3]; PIN(t_); bcc[2][15] = t_; } SBAR();
    SBAR();
    pk0[2] = cvtpk(bcc[2][4], bcc[2][5]);
    SBAR();
    pk0[3] = cvtpk(bcc[2][6], bcc[2][7]); PIN(pk0);
    SBAR();
    bcc[4] = MFMA(wf6, ho[6], bcc[4]);
    SBAR();
    wf10 = trfrag(trB0 + 40960, 1024);
    SBAR();
    pk1[0] = cvtpk(bcc[2][8], bcc[2][9]);
    SBAR();
    pk1[1] = cvtpk(bcc[2][10], bcc[2][11]);
    SBAR();
    stpart(pk0, ROT3(1) * 114688); stu4(kp, pk0);
    SBAR();
    bcc[4] = MFMA(wf7, ho[7], bcc[4]);
    SBAR();
    wf11 = trfrag(trB0 + 43008, 1024);
    SBAR();
    pk1[2] = cvtpk(bcc[2][12], bcc[2][13]);
    SBAR();
    pk1[3] = cvtpk(bcc[2][14], bcc[2][15]); PIN(pk1);
    SBAR();
    xw += 98304; PIN(xw); *(LAS f32x4*)(xw + 0) = (f32x4){bcc[5][0], bcc[5][1], bcc[5][2], bcc[5][3]};
    SBAR();
    bcc[4] = MFMA(wf8, ho[8], bcc[4]);
    SBAR();
    stpart(pk1, ROT3(1) * 114688 + 1024); stu4(kp + 1024, pk1); kp += kstep; PIN(kp);
    SBAR();
    *(LAS f32x4*)(xw + 1024) = (f32x4){bcc[5][4], bcc[5][5], bcc[5][6], bcc[5][7]};
    SBAR();
    bcc[4] = MFMA(wf9, ho[9], bcc[4]);
    SBAR();
    *(LAS f32x4*)(xw + 2048) = (f32x4){bcc[5][8], bcc[5][9], bcc[5][10], bcc[5][11]};
    SBAR();
    bcc[4] = MFMA(wf10, ho[10], bcc[4]);
    SBAR();
    *(LAS f32x4*)(xw + 3072) = (f32x4){bcc[5][12], bcc[5][13], bcc[5][14], bcc[5][15]};
    SBAR();
    bcc[4] = MFMA(wf11, ho[11], bcc[4]);
    SBAR();
    asm volatile("s_waitcnt lgkmcnt(0)\n\ts_barrier" ::: "memory"); SBAR();
    xrd += 98304; PIN(xrd); xv0 = ldf4(xrd + 0);
    xv1 = ldf4(xrd + 1024);
    xv2 = ldf4(xrd + 2048);
    xv3 = ldf4(xrd + 3072);
    { float t_ = bcc[4][0] + xv0[0]; PIN(t_); bcc[4][0] = t_; } SBAR(); { float t_ = bcc[4][1] + xv0[1]; PIN(t_); bcc[4][1] = t_; } SBAR(); { float t_ = bcc[4][2] + xv0[2]; PIN(t_); bcc[4][2] = t_; } SBAR(); { float t_ = bcc[4][3] + xv0[3]; PIN(t_); bcc[4][3] = t_; } SBAR();
    { float t_ = bcc[4][4] + xv1[0]; PIN(t_); bcc[4][4] = t_; } SBAR(); { float t_ = bcc[4][5] + xv1[1]; PIN(t_); bcc[4][5] = t_; } SBAR(); { float t_ = bcc[4][6] + xv1[2]; PIN(t_); bcc[4][6] = t_; } SBAR(); { float t_ = bcc[4][7] + xv1[3]; PIN(t_); bcc[4][7] = t_; } SBAR();
    { float t_ = bcc[4][8] + xv2[0]; PIN(t_); bcc[4][8] = t_; } SBAR(); { float t_ = bcc[4][9] + xv2[1]; PIN(t_); bcc[4][9] = t_; } SBAR(); { float t_ = bcc[4][10] + xv2[2]; PIN(t_); bcc[4][10] = t_; } SBAR(); { float t_ = bcc[4][11] + xv2[3]; PIN(t_); bcc[4][11] = t_; } SBAR();
    pk0[0] = cvtpk(bcc[4][0], bcc[4][1]);
    pk0[1] = cvtpk(bcc[4][2], bcc[4][3]);
    { float t_ = bcc[4][12] + xv3[0]; PIN(t_); bcc[4][12] = t_; } SBAR(); { float t_ = bcc[4][13] + xv3[1]; PIN(t_); bcc[4][13] = t_; } SBAR(); { float t_ = bcc[4][14] + xv3[2]; PIN(t_); bcc[4][14] = t_; } SBAR(); { float t_ = bcc[4][15] + xv3[3]; PIN(t_); bcc[4][15] = t_; } SBAR();
    pk0[2] = cvtpk(bcc[4][4], bcc[4][5]);
    pk0[3] = cvtpk(bcc[4][6], bcc[4][7]); PIN(pk0);
    pk1[0] = cvtpk(bcc[4][8], bcc[4][9]);
    pk1[1] = cvtpk(bcc[4][10], bcc[4][11]);
    stpart(pk0, ROT3(2) * 114688); stu4(kp, pk0);
    pk1[2] = cvtpk(bcc[4][12], bcc[4][13]);
    pk1[3] = cvtpk(bcc[4][14], bcc[4][15]); PIN(pk1);
    stpart(pk1, ROT3(2) * 114688 + 1024); stu4(kp + 1024, pk1); kp += kstep; PIN(kp);
        f_ct = fct; f_jj = fjj;
#undef FS11
#undef PF2
    } else {
        ROLE_IDS(); const int sl = slice - 12;
        const char* gbr = bre + (size_t)sl * (192 * 1152); const char* gbi = bim + (size_t)sl * (192 * 1152);
        const char* gcr = cre + sl * 384; const char* gci = cim + sl * 384;
        const int tok = 16 * ((l31 >> 2) & 1) + (l31 & 3) + 4 * (l31 >> 3);
        const int xr = 32 * (rg & 1) + tok; const unsigned rxo = (rg >> 1) * SLOT + xr * 128 + ((h ^ ((xr >> 1) & 7)) << 4);
        lptr rx0 = L + rxo, rx1 = L + (rxo ^ 32u), rx2 = L + (rxo ^ 64u), rx3 = L + (rxo ^ 96u);
        const int wr = 32 * cg + l31; const unsigned rbo = wr * 128 + ((h ^ ((wr >> 1) & 7)) << 4);
        lptr rb0 = L + rbo, rb1 = L + (rbo ^ 32u), rb2 = L + (rbo ^ 64u), rb3 = L + (rbo ^ 96u), rbCS = L + (rbo ^ (rg << 5));
        constexpr int S_H = 5 * SLOT, S_E = S_H + 32768, S_RT = S_E + 4096, S_PR = S_RT + 512;
        int seg = 2 * rg + h; PIN(seg);
        lptr sq = L + S_H + wr * 8, pw = sq + seg * 512, pt, ht0, ht1; PIN(sq); PIN(pw);
        unsigned hwo = S_H + rg * 8192 + wr * 64 + (((2 * h) ^ ((l31 >> 1) & 3)) << 4); PIN(hwo);
#define HW(g) (L + (hwo ^ ((g) << 4)))
#define TBF(v, k) __uint_as_float(v[k])
#define MK_PT() do { int s_ = seg; PIN(s_); pt = L + S_RT + s_ * 64; PIN(pt); } while (0)
#define MK_S1() do { int s_ = seg; PIN(s_); voffS1 = voffC + s_ * 3072; } while (0)
#define MK_HT() do { LANE2(l_); const unsigned o_ = S_H + rg * 8192 + (8 * (l_ >> 5) + ((l_ & 15) >> 2)) * 64 + (((2 * ((l_ >> 4) & 1) + ((l_ & 3) >> 1)) ^ ((l_ & 15) >> 3)) << 4) + 8 * (l_ & 1); \
        ht0 = L + o_; ht1 = L + ((o_ + 256) ^ 32u); PIN(ht0); PIN(ht1); } while (0)
#define PUBLISH(q) do { if (rg == 0 && rt < 15 && seg == 0) st16((u32x4){__float_as_uint(Pr), tag, __float_as_uint(Pi), tag}, \
        __builtin_amdgcn_make_buffer_rsrc((void*)(ggr + (q) * 16384), 0, 16384, 0x00020000), voffC, rt * 1024, 16); } while (0)
#define LB_ADDR() do { int s_ = seg; PIN(s_); const unsigned r0_ = rt - 1 - 2 * s_, r1_ = r0_ - 1; lbo0 = (r0_ < 15u ? r0_ : 15u) * 1024 + voffC; lbo1 = (r1_ < 15u ? r1_ : 15u) * 1024 + voffC; } while (0)
#define LB_CHECK(q) do { if (__builtin_expect(__any(g0[1] != tag || g0[3] != tag || g1[1] != tag || g1[3] != tag), 0)) { LB_ADDR(); g0 = repoll4(ggr + (q) * 16384, lbo0, tag, true); g1 = repoll4(ggr + (q) * 16384, lbo1, tag, true); } } while (0)
        f32x16 acc2, bcc[9]; f32x2 prm, pfl, pfg, rt40, rt41; unsigned hlr[16], hli[16], or0, or1, or2, or3, oi0, oi1, oi2, oi3, lbo0, lbo1, voffS1; bf16x8 hs[16];
        float hr0, hi0, hr1, hi1, vr0, vi0, vr1, vi1, Pwr, Pwi, ua, ub, Pr, Pi, Cr, Ci, zr0, zi0, zr1, zi1, ur0, ui0, ur1, ui1, t0_, t1_, t2_, t3_;
        f32x2 ea0, eb0, ea1, eb1, fa0, fb0, fa1, fb1; u32x4 g0, g1, tb0, tb1, tc;
        const char* gpr = ws + WS_PRM + sl * PRM_SLICE; const unsigned voffP = ((wave < 3 ? wave : 2) * 64 + lane) * 16;
        const char* ggr = ws + WS_GRAN + sl * 49152;
#define SSM_PARAMS() do { if (__builtin_expect(__any(g0[1] != tag || g0[3] != tag || g1[1] != tag || g1[3] != tag), 0)) { g0 = repoll4(gpr, voffP, tag, true); g1 = repoll4(gpr + 3072, voffP, tag, true); } \
        stf2(L + S_PR + (voffP >> 1), TBF(g0, 0), TBF(g0, 2)); stf2(L + S_PR + 1536 + (voffP >> 1), TBF(g1, 0), TBF(g1, 2)); } while (0)
        const unsigned voffT = (rg * 32 + tok) * 8 + h * 3072;
#define FS11() do { FS11A(); stf32(L + S_RT + ((voffT & 1023) >> 1), rr); } while (0)
    ALD1(ep, 0u, ws + WS_EPOCH);
    GL(gbr, kx0 + 0, voffR, 0);
    GL(gx, kx0 + 128, voffR, 106496);
    GL(gx, kx0 + 73856, voffR, 131072);
    GL(gbr, kx0 + 128, voffR, 8192);
    GL(gx, kx0 + 256, voffR, 114688);
    GL(gx, kx0 + 73984, voffR, 139264);
    GL(gbr, kx0 + 256, voffR, 16384);
    asm volatile("s_waitcnt vmcnt(6) lgkmcnt(0)\n\ts_barrier" ::: "memory"); SBAR();
    rx0 += 98304; PIN(rx0); xf[0] = ldb128(rx0 + 0);
    wf0 = ldb128(rb0 + 0);
    rx1 += 98304; PIN(rx1); xf[1] = ldb128(rx1 + 0);
    SBAR();
    GL(gx, kx1 + 0, voffR, 24576);
    SBAR();
    wf1 = ldb128(rb1 + 0);
    rx2 += 98304; PIN(rx2); xf[2] = ldb128(rx2 + 0);
    wf2 = ldb128(rb2 + 0);
    SBAR();
    GL(gx, kx1 + 73728, voffR, 49152);
    SBAR();
    rx3 += 98304; PIN(rx3); xf[3] = ldb128(rx3 + 0);
    wf3 = ldb128(rb3 + 0);
    cf0 = ldb128(rbCS + 0);
    SBAR();
    GL(gbr, kx1 + 0, voffR, 73728);
    SBAR();
    GL(gx, kx1 + 128, voffR, 32768);
    SBAR();
    asm volatile("; VW %0" : "+v"(ep)); GOT_EPOCH();
    asm volatile("s_waitcnt vmcnt(7) lgkmcnt(0)\n\ts_barrier" ::: "memory"); SBAR();
    acc0 = MFMA(xf[0], wf0, zero16);
    SBAR();
    xf[4] = ldb128(rx0 + 8192);
    wf4 = ldb128(rb0 + 8192);
    xf[5] = ldb128(rx1 + 8192);
    SBAR();
    GL(gx, kx1 + 73856, voffR, 57344);
    SBAR();
    acc0 = MFMA(xf[1], wf1, acc0);
    SBAR();
    wf5 = ldb128(rb1 + 8192);
    xf[6] = ldb128(rx2 + 8192);
    wf6 = ldb128(rb2 + 8192);
    SBAR();
    GL(gbr, kx1 + 128, voffR, 81920);
    SBAR();
    acc0 = MFMA(xf[2], wf2, acc0);
    SBAR();
    xf[7] = ldb128(rx3 + 8192);
    wf7 = ldb128(rb3 + 8192);
    cf1 = ldb128(rbCS + 8192);
    SBAR();
    GL(gx, kx1 + 256, voffR, 40960);
    SBAR();
    acc0 = MFMA(xf[3], wf3, acc0);
    SBAR();
    GL(gx, kx1 + 73984, voffR, 65536);
    SBAR();
    csa = MFMA(ones8, cf0, zero16);
    SBAR();
    asm volatile("s_waitcnt vmcnt(8) lgkmcnt(0)\n\ts_barrier" ::: "memory"); SBAR();
    acc0 = MFMA(xf[4], wf4, acc0);
    SBAR();
    xf[8] = ldb128(rx0 + 16384);
    wf8 = ldb128(rb0 + 16384);
    xf[9] = ldb128(rx1 + 16384);
    SBAR();
    GL(gbr, kx1 + 256, voffR, 90112);
    SBAR();
    acc0 = MFMA(xf[5], wf5, acc0);
    SBAR();
    wf9 = ldb128(rb1 + 16384);
    xf[10] = ldb128(rx2 + 16384);
    wf10 = ldb128(rb2 + 16384);
    SBAR();
    GL(gx, kx2 + 0, voffR, 98304);
    SBAR();
    acc0 = MFMA(xf[6], wf6, acc0);
    SBAR();
    xf[11] = ldb128(rx3 + 16384);
    wf11 = ldb128(rb3 + 16384);
    cf0 = ldb128(rbCS + 16384);
    SBAR();
    GL(gx, kx2 + 73728, voffR, 122880);
    SBAR();
    acc0 = MFMA(xf[7], wf7, acc0);
    SBAR();
    GL(gbr, kx2 + 0, voffR, 0);
    SBAR();
    csa = MFMA(ones8, cf1, csa);
    SBAR();
    asm volatile("s_waitcnt vmcnt(9) lgkmcnt(0)\n\ts_barrier" ::: "memory"); SBAR();
    acc0 = MFMA(xf[8], wf8, acc0);
    SBAR();
    rx0 += -73728; PIN(rx0); xf[12] = ldb128(rx0 + 0);
    rb0 += 73728; PIN(rb0); wf0 = ldb128(rb0 + 0);
    rx1 += -73728; PIN(rx1); xf[13] = ldb128(rx1 + 0);
    SBAR();
    GL(gx, kx2 + 128, voffR, 106496);
    SBAR();
    acc0 = MFMA(xf[9], wf9, acc0);
    SBAR();
    rb1 += 73728; PIN(rb1); wf1 = ldb128(rb1 + 0);
    rx2 += -73728; PIN(rx2); xf[14] = ldb128(rx2 + 0);
    rb2 += 73728; PIN(rb2); wf2 = ldb128(rb2 + 0);
    SBAR();
    GL(gx, kx2 + 73856, voffR, 131072);
    SBAR();
    acc0 = MFMA(xf[10], wf10, acc0);
    SBAR();
    rx3 += -73728; PIN(rx3); xf[15] = ldb128(rx3 + 0);
    rb3 += 73728; PIN(rb3); wf3 = ldb128(rb3 + 0);
    rbCS += 73728; PIN(rbCS); cf1 = ldb128(rbCS + 0);
    SBAR();
    GL(gbr, kx2 + 128, voffR, 8192);
    SBAR();
    acc0 = MFMA(xf[11], wf11, acc0);
    SBAR();
    GL(gx, kx2 + 256, voffR, 114688);
    SBAR();
    csa = MFMA(ones8, cf0, csa);
    SBAR();
    asm volatile("s_waitcnt vmcnt(10) lgkmcnt(0)\n\ts_barrier" ::: "memory"); SBAR();
    acc0 = MFMA(xf[12], wf0, acc0);
    SBAR();
    xf[16] = ldb128(rx0 + 8192);
    wf4 = ldb128(rb0 + 8192);
    xf[17] = ldb128(rx1 + 8192);
    SBAR();
    GL(gx, kx2 + 73984, voffR, 139264);
    SBAR();
    acc0 = MFMA(xf[13], wf1, acc0);
    SBAR();
    wf5 = ldb128(rb1 + 8192);
    xf[18] = ldb128(rx2 + 8192);
    wf6 = ldb128(rb2 + 8192);
    SBAR();
    GL(gbr, kx2 + 256, voffR, 16384);
    SBAR();
    acc0 = MFMA(xf[14], wf2, acc0);
    SBAR();
    xf[19] = ldb128(rx3 + 8192);
    wf7 = ldb128(rb3 + 8192);
    cf0 = ldb128(rbCS + 8192);
    SBAR();
    GL(gbi, kx0 + 0, voffR, 24576);
    SBAR();
    acc0 = MFMA(xf[15], wf3, acc0);
    SBAR();
    csa = MFMA(ones8, cf1, csa);
    SBAR();
    asm volatile("s_waitcnt vmcnt(10) lgkmcnt(0)\n\ts_barrier" ::: "memory"); SBAR();
    acc0 = MFMA(xf[16], wf4, acc0);
    SBAR();
    xf[20] = ldb128(rx0 + 16384);
    wf8 = ldb128(rb0 + 16384);
    xf[21] = ldb128(rx1 + 16384);
    SBAR();
    GL(gbi, kx0 + 128, voffR, 32768);
    SBAR();
    acc0 = MFMA(xf[17], wf5, acc0);
    SBAR();
    wf9 = ldb128(rb1 + 16384);
    xf[22] = ldb128(rx2 + 16384);
    wf10 = ldb128(rb2 + 16384);
    SBAR();
    acc0 = MFMA(xf[18], wf6, acc0);
    SBAR();
    xf[23] = ldb128(rx3 + 16384);
    wf11 = ldb128(rb3 + 16384);
    cf1 = ldb128(rbCS + 16384);
    SBAR();
    acc0 = MFMA(xf[19], wf7, acc0);
    SBAR();
    csa = MFMA(ones8, cf0, csa);
    SBAR();
    asm volatile("s_waitcnt vmcnt(8) lgkmcnt(0)\n\ts_barrier" ::: "memory"); SBAR();
    acc0 = MFMA(xf[20], wf8, acc0);
    SBAR();
    rx0 += 73728; PIN(rx0); xf[24] = ldb128(rx0 + 0);
    rb0 += -73728; PIN(rb0); wf0 = ldb128(rb0 + 0);
    rx1 += 73728; PIN(rx1); xf[25] = ldb128(rx1 + 0);
    SBAR();
    GL(gbi, kx0 + 256, voffR, 40960);
    SBAR();
    acc0 = MFMA(xf[21], wf9, acc0);
    SBAR();
    rb1 += -73728; PIN(rb1); wf1 = ldb128(rb1 + 0);
    rx2 += 73728; PIN(rx2); xf[26] = ldb128(rx2 + 0);
    rb2 += -73728; PIN(rb2); wf2 = ldb128(rb2 + 0);
    SBAR();
    GL(gbi, kx1 + 0, voffR, 49152);
    SBAR();
    acc0 = MFMA(xf[22], wf10, acc0);
    SBAR();
    rx3 += 73728; PIN(rx3); xf[27] = ldb128(rx3 + 0);
    rb3 += -73728; PIN(rb3); wf3 = ldb128(rb3 + 0);
    rbCS += -73728; PIN(rbCS); cf0 = ldb128(rbCS + 0);
    SBAR();
    GL(gbi, kx1 + 128, voffR, 57344);
    SBAR();
    acc0 = MFMA(xf[23], wf11, acc0);
    SBAR();
    GL(gbi, kx1 + 256, voffR, 65536);
    SBAR();
    csa = MFMA(ones8, cf1, csa);
    SBAR();
    asm volatile("s_waitcnt vmcnt(9) lgkmcnt(0)\n\ts_barrier" ::: "memory"); SBAR();
    acc0 = MFMA(xf[24], wf0, acc0);
    SBAR();
    xf[28] = ldb128(rx0 + 8192);
    wf4 = ldb128(rb0 + 8192);
    xf[29] = ldb128(rx1 + 8192);
    SBAR();
    GL(gbi, kx2 + 0, voffR, 73728);
    SBAR();
    acc0 = MFMA(xf[25], wf1, acc0);
    SBAR();
    wf5 = ldb128(rb1 + 8192);
    xf[30] = ldb128(rx2 + 8192);
    wf6 = ldb128(rb2 + 8192);
    SBAR();
    GL(gbi, kx2 + 128, voffR, 81920);
    SBAR();
    acc0 = MFMA(xf[26], wf2, acc0);
    SBAR();
    xf[31] = ldb128(rx3 + 8192);
    wf7 = ldb128(rb3 + 8192);
    cf1 = ldb128(rbCS + 8192);
    SBAR();
    GL(gbi, kx2 + 256, voffR, 90112);
    SBAR();
    acc0 = MFMA(xf[27], wf3, acc0);
    SBAR();
    GL(gbr, kx0 + 73728, voffR, 98304);
    SBAR();
    csa = MFMA(ones8, cf0, csa);
    SBAR();
    asm volatile("s_waitcnt vmcnt(10) lgkmcnt(0)\n\ts_barrier" ::: "memory"); SBAR();
    acc0 = MFMA(xf[28], wf4, acc0);
    SBAR();
    cf0 = ldb128(rbCS + 16384);
    xf[32] = ldb128(rx0 + 16384);
    wf8 = ldb128(rb0 + 16384);
    SBAR();
    GL(gbr, kx0 + 73856, voffR, 106496);
    SBAR();
    acc0 = MFMA(xf[29], wf5, acc0);
    SBAR();
    xf[33] = ldb128(rx1 + 16384);
    wf9 = ldb128(rb1 + 16384);
    xf[34] = ldb128(rx2 + 16384);
    SBAR();
    acc0 = MFMA(xf[30], wf6, acc0);
    SBAR();
    wf10 = ldb128(rb2 + 16384);
    xf[35] = ldb128(rx3 + 16384);
    wf11 = ldb128(rb3 + 16384);
    SBAR();
    acc0 = MFMA(xf[31], wf7, acc0);
    SBAR();
    csa = MFMA(ones8, cf1, csa);
    SBAR();
    csa = MFMA(ones8, cf0, csa); SBAR(); stf32(L + AUX + 2048 + voffC + rg * 4, csa[0]); SBAR();
    asm volatile("s_waitcnt vmcnt(5) lgkmcnt(0)\n\ts_barrier" ::: "memory"); SBAR();
    wf0 = ldb128(rb0 + 24576);
    wf1 = ldb128(rb1 + 24576);
    SBAR();
    csv = ldf4(L + AUX + 2048 + voffC);
    acc0 = MFMA(xf[32], wf8, acc0);
    SBAR();
    acc0 = MFMA(xf[33], wf9, acc0);
    SBAR();
    acc0 = MFMA(xf[34], wf10, acc0);
    SBAR();
    cor2 = mkcorr((csv[0] + csv[1]) + (csv[2] + csv[3]), 0u, 0u); PIN(cor2);
    SBAR();
    acc0 = MFMA(xf[35], wf11, acc0);
    SBAR();
    acc1 = MFMA(xf[0], wf0, zero16);
    SBAR();
    wf2 = ldb128(rb2 + 24576);
    SBAR();
    ALD2(sg0, voffT, gps); ALD2(sg1, voffT, gps + 1024); ALD2(cv, voffT, gps + 2048);
    SBAR();
    acc1 = MFMA(xf[1], wf1, acc1);
    SBAR();
    wf3 = ldb128(rb3 + 24576);
    SBAR();
    GL(gbr, kx0 + 73984, voffR, 114688);
    SBAR();
    acc1 = MFMA(xf[2], wf2, acc1);
    SBAR();
    wf4 = ldb128(rb0 + 32768);
    SBAR();
    acc1 = MFMA(xf[3], wf3, acc1);
    SBAR();
    wf5 = ldb128(rb1 + 32768);
    SBAR();
    acc1 = MFMA(xf[4], wf4, acc1);
    SBAR();
    wf6 = ldb128(rb2 + 32768);
    SBAR();
    GL(gbr, kx1 + 73728, voffR, 0);
    SBAR();
    acc1 = MFMA(xf[5], wf5, acc1);
    SBAR();
    wf7 = ldb128(rb3 + 32768);
    SBAR();
    acc1 = MFMA(xf[6], wf6, acc1);
    SBAR();
    wf8 = ldb128(rb0 + 40960);
    SBAR();
    acc1 = MFMA(xf[7], wf7, acc1);
    SBAR();
    wf9 = ldb128(rb1 + 40960);
    SBAR();
    GL(gbr, kx1 + 73856, voffR, 8192);
    SBAR();
    acc1 = MFMA(xf[8], wf8, acc1);
    SBAR();
    wf10 = ldb128(rb2 + 40960);
    SBAR();
    acc1 = MFMA(xf[9], wf9, acc1);
    SBAR();
    wf11 = ldb128(rb3 + 40960);
    SBAR();
    ALD4(g0, voffP, gpr); ALD4(g1, voffP, gpr + 3072);
    SBAR();
    acc1 = MFMA(xf[10], wf10, acc1);
    SBAR();
    rb0 += 49152; PIN(rb0); wf0 = ldb128(rb0 + 0);
    rb1 += 49152; PIN(rb1); wf1 = ldb128(rb1 + 0);
    SBAR();
    GL(gbr, kx1 + 73984, voffR, 16384);
    SBAR();
    acc1 = MFMA(xf[11], wf11, acc1);
    SBAR();
    ALD4(cor, voffC, gco + 1024);
    SBAR();
    asm volatile("s_waitcnt vmcnt(12)\n\ts_barrier" ::: "memory"); SBAR();
    acc1 = MFMA(xf[12], wf0, acc1);
    SBAR();
    rb2 += 49152; PIN(rb2); wf2 = ldb128(rb2 + 0);
    SBAR();
    acc1 = MFMA(xf[13], wf1, acc1);
    SBAR();
    rb3 += 49152; PIN(rb3); wf3 = ldb128(rb3 + 0);
    SBAR();
    GL(gbr, kx2 + 73728, voffR, 24576);
    SBAR();
    acc1 = MFMA(xf[14], wf2, acc1);
    SBAR();
    wf4 = ldb128(rb0 + 8192);
    SBAR();
    acc1 = MFMA(xf[15], wf3, acc1);
    SBAR();
    wf5 = ldb128(rb1 + 8192);
    SBAR();
    acc1 = MFMA(xf[16], wf4, acc1);
    SBAR();
    wf6 = ldb128(rb2 + 8192);
    SBAR();
    acc1 = MFMA(xf[17], wf5, acc1);
    SBAR();
    wf7 = ldb128(rb3 + 8192);
    SBAR();
    GL(gbr, kx2 + 73856, voffR, 32768);
    SBAR();
    acc1 = MFMA(xf[18], wf6, acc1);
    SBAR();
    wf8 = ldb128(rb0 + 16384);
    SBAR();
    acc1 = MFMA(xf[19], wf7, acc1);
    SBAR();
    wf9 = ldb128(rb1 + 16384);
    SBAR();
    acc1 = MFMA(xf[20], wf8, acc1);
    SBAR();
    wf10 = ldb128(rb2 + 16384);
    SBAR();
    acc1 = MFMA(xf[21], wf9, acc1);
    SBAR();
    wf11 = ldb128(rb3 + 16384);
    SBAR();
    GL(gbr, kx2 + 73984, voffR, 40960);
    SBAR();
    acc1 = MFMA(xf[22], wf10, acc1);
    SBAR();
    wf0 = ldb128(rb0 + 24576);
    wf1 = ldb128(rb1 + 24576);
    SBAR();
    acc1 = MFMA(xf[23], wf11, acc1);
    SBAR();
    asm volatile("s_waitcnt vmcnt(9)\n\ts_barrier" ::: "memory"); SBAR();
    acc1 = MFMA(xf[24], wf0, acc1);
    SBAR();
    wf2 = ldb128(rb2 + 24576);
    SBAR();
    VWAIT3(10, sg0, sg1, cv); CHK_STAT();
    SBAR();
    acc1 = MFMA(xf[25], wf1, acc1);
    SBAR();
    wf3 = ldb128(rb3 + 24576);
    SBAR();
    FS1();
    SBAR();
    FS2();
    SBAR();
    GL(gbi, kx0 + 73728, voffR, 49152);
    SBAR();
    acc1 = MFMA(xf[26], wf2, acc1);
    SBAR();
    wf4 = ldb128(rb0 + 32768);
    SBAR();
    FS3();
    SBAR();
    FS4();
    SBAR();
    acc1 = MFMA(xf[27], wf3, acc1);
    SBAR();
    wf5 = ldb128(rb1 + 32768);
    SBAR();
    FS5();
    SBAR();
    FS6();
    SBAR();
    acc1 = MFMA(xf[28], wf4, acc1);
    SBAR();
    wf6 = ldb128(rb2 + 32768);
    SBAR();
    FS7();
    SBAR();
    FS8();
    SBAR();
    acc1 = MFMA(xf[29], wf5, acc1);
    SBAR();
    wf7 = ldb128(rb3 + 32768);
    SBAR();
    FS9();
    SBAR();
    FS10();
    SBAR();
    GL(gbi, kx0 + 73856, voffR, 57344);
    SBAR();
    acc1 = MFMA(xf[30], wf6, acc1);
    SBAR();
    wf8 = ldb128(rb0 + 40960);
    SBAR();
    FS11();
    SBAR();
    acc1 = MFMA(xf[31], wf7, acc1);
    SBAR();
    wf9 = ldb128(rb1 + 40960);
    SBAR();
    VWAIT1(5, cor); CHK_COR(cor, 1);
    SBAR();
    acc1 = MFMA(xf[32], wf8, acc1);
    SBAR();
    wf10 = ldb128(rb2 + 40960);
    SBAR();
    VWAIT2(7, g0, g1); SSM_PARAMS();
    SBAR();
    acc0 = MFMA(lnf, CORF2, acc0);
    SBAR();
    acc1 = MFMA(xf[33], wf9, acc1);
    SBAR();
    wf11 = ldb128(rb3 + 40960);
    SBAR();
    MK_PT();
    SBAR();
    GL(gbi, kx0 + 73984, voffR, 65536);
    SBAR();
    acc1 = MFMA(lnf, CORF, acc1);
    SBAR();
    acc1 = MFMA(xf[34], wf10, acc1);
    SBAR();
    rb0 += 49152; PIN(rb0); wf0 = ldb128(rb0 + 0);
    rb1 += 49152; PIN(rb1); wf1 = ldb128(rb1 + 0);
    SBAR();
    acc1 = MFMA(xf[35], wf11, acc1);
    SBAR();
    asm volatile("s_waitcnt vmcnt(7) lgkmcnt(2)\n\ts_barrier" ::: "memory"); SBAR();
    acc2 = MFMA(xf[0], wf0, zero16);
    SBAR();
    rb2 += 49152; PIN(rb2); wf2 = ldb128(rb2 + 0);
    SBAR();
    prm = ldf2(sq + 37376);
    SBAR();
    rt40 = ldf2(pt + 0);
    SBAR();
    acc2 = MFMA(xf[1], wf1, acc2);
    SBAR();
    rb3 += 49152; PIN(rb3); wf3 = ldb128(rb3 + 0);
    SBAR();
    GL(gbi, kx1 + 73728, voffR, 73728);
    SBAR();
    acc2 = MFMA(xf[2], wf2, acc2);
    SBAR();
    wf4 = ldb128(rb0 + 8192);
    SBAR();
    hr0 = acc0[0] * rt40[0]; PIN(hr0); SBAR(); hi0 = acc1[0] * rt40[0]; PIN(hi0); SBAR();
    SBAR();
    rt41 = ldf2(pt + 8);
    SBAR();
    acc2 = MFMA(xf[3], wf3, acc2);
    SBAR();
    wf5 = ldb128(rb1 + 8192);
    SBAR();
    vr1 = acc0[1] * rt40[1]; PIN(vr1); SBAR(); vi1 = acc1[1] * rt40[1]; PIN(vi1); SBAR();
    SBAR();
    acc2 = MFMA(xf[4], wf4, acc2);
    SBAR();
    wf6 = ldb128(rb2 + 8192);
    SBAR();
    vi1 = FMA(prm[1], hr0, vi1); PIN(vi1); SBAR(); vr1 = FMA(-prm[1], hi0, vr1); PIN(vr1); SBAR();
    SBAR();
    vr0 = acc0[2] * rt41[0]; PIN(vr0); SBAR(); vi0 = acc1[2] * rt41[0]; PIN(vi0); SBAR();
    SBAR();
    rt40 = ldf2(pt + 16);
    SBAR();
    acc2 = MFMA(xf[5], wf5, acc2);
    SBAR();
    wf7 = ldb128(rb3 + 8192);
    SBAR();
    hi1 = FMA(prm[0], hi0, vi1); PIN(hi1); SBAR(); hr1 = FMA(prm[0], hr0, vr1); PIN(hr1); SBAR();
    SBAR();
    hli[0] = cvtpk(hi0, hi1); PIN(hli[0]); SBAR(); hlr[0] = cvtpk(hr0, hr1); PIN(hlr[0]); SBAR();
    SBAR();
    vi0 = FMA(prm[1], hr1, vi0); PIN(vi0); SBAR(); vr0 = FMA(-prm[1], hi1, vr0); PIN(vr0); SBAR();
    SBAR();
    vr1 = acc0[3] * rt41[1]; PIN(vr1); SBAR(); vi1 = acc1[3] * rt41[1]; PIN(vi1); SBAR();
    SBAR();
    GL(gbi, kx1 + 73856, voffR, 81920);
    SBAR();
    acc2 = MFMA(xf[6], wf6, acc2);
    SBAR();
    wf8 = ldb128(rb0 + 16384);
    SBAR();
    hr0 = FMA(prm[0], hr1, vr0); PIN(hr0); SBAR(); hi0 = FMA(prm[0], hi1, vi0); PIN(hi0); SBAR();
    SBAR();
    vi1 = FMA(prm[1], hr0, vi1); PIN(vi1); SBAR(); vr1 = FMA(-prm[1], hi0, vr1); PIN(vr1); SBAR();
    SBAR();
    vr0 = acc0[4] * rt40[0]; PIN(vr0); SBAR(); vi0 = acc1[4] * rt40[0]; PIN(vi0); SBAR();
    SBAR();
    rt41 = ldf2(pt + 24);
    SBAR();
    acc2 = MFMA(xf[7], wf7, acc2);
    SBAR();
    wf9 = ldb128(rb1 + 16384);
    SBAR();
    hi1 = FMA(prm[0], hi0, vi1); PIN(hi1); SBAR(); hr1 = FMA(prm[0], hr0, vr1); PIN(hr1); SBAR();
    SBAR();
    hli[1] = cvtpk(hi0, hi1); PIN(hli[1]); SBAR(); hlr[1] = cvtpk(hr0, hr1); PIN(hlr[1]); SBAR();
    SBAR();
    vi0 = FMA(prm[1], hr1, vi0); PIN(vi0); SBAR(); vr0 = FMA(-prm[1], hi1, vr0); PIN(vr0); SBAR();
    SBAR();
    vr1 = acc0[5] * rt40[1]; PIN(vr1); SBAR(); vi1 = acc1[5] * rt40[1]; PIN(vi1); SBAR();
    SBAR();
    acc2 = MFMA(xf[8], wf8, acc2);
    SBAR();
    wf10 = ldb128(rb2 + 16384);
    SBAR();
    MK_S1(); ALD4(tb0, voffS1, gpr + 9216);
    SBAR();
    hr0 = FMA(prm[0], hr1, vr0); PIN(hr0); SBAR(); hi0 = FMA(prm[0], hi1, vi0); PIN(hi0); SBAR();
    SBAR();
    vi1 = FMA(prm[1], hr0, vi1); PIN(vi1); SBAR(); vr1 = FMA(-prm[1], hi0, vr1); PIN(vr1); SBAR();
    SBAR();
    vr0 = acc0[6] * rt41[0]; PIN(vr0); SBAR(); vi0 = acc1[6] * rt41[0]; PIN(vi0); SBAR();
    SBAR();
    acc2 = MFMA(xf[9], wf9, acc2);
    SBAR();
    wf11 = ldb128(rb3 + 16384);
    SBAR();
    hi1 = FMA(prm[0], hi0, vi1); PIN(hi1); SBAR(); hr1 = FMA(prm[0], hr0, vr1); PIN(hr1); SBAR();
    SBAR();
    hli[2] = cvtpk(hi0, hi1); PIN(hli[2]); SBAR(); hlr[2] = cvtpk(hr0, hr1); PIN(hlr[2]); SBAR();
    SBAR();
    vi0 = FMA(prm[1], hr1, vi0); PIN(vi0); SBAR(); vr0 = FMA(-prm[1], hi1, vr0); PIN(vr0); SBAR();
    SBAR();
    vr1 = acc0[7] * rt41[1]; PIN(vr1); SBAR(); vi1 = acc1[7] * rt41[1]; PIN(vi1); SBAR();
    SBAR();
    rt40 = ldf2(pt + 32);
    SBAR();
    GL(gbi, kx1 + 73984, voffR, 90112);
    SBAR();
    acc2 = MFMA(xf[10], wf10, acc2);
    SBAR();
    rb0 += -98304; PIN(rb0); wf0 = ldb128(rb0 + 0);
    rb1 += -98304; PIN(rb1); wf1 = ldb128(rb1 + 0);
    SBAR();
    hr0 = FMA(prm[0], hr1, vr0); PIN(hr0); SBAR(); hi0 = FMA(prm[0], hi1, vi0); PIN(hi0); SBAR();
    SBAR();
    vi1 = FMA(prm[1], hr0, vi1); PIN(vi1); SBAR(); vr1 = FMA(-prm[1], hi0, vr1); PIN(vr1); SBAR();
    SBAR();
    acc2 = MFMA(xf[11], wf11, acc2);
    SBAR();
    hi1 = FMA(prm[0], hi0, vi1); PIN(hi1); SBAR(); hr1 = FMA(prm[0], hr0, vr1); PIN(hr1); SBAR();
    SBAR();
    hli[3] = cvtpk(hi0, hi1); PIN(hli[3]); SBAR(); hlr[3] = cvtpk(hr0, hr1); PIN(hlr[3]); SBAR();
    SBAR();
    stu4(HW(0) + 4096, (u32x4){hli[0], hli[1], hli[2], hli[3]});
    SBAR();
    vr0 = acc0[8] * rt40[0]; PIN(vr0); SBAR(); vi0 = acc1[8] * rt40[0]; PIN(vi0); SBAR();
    SBAR();
    rt41 = ldf2(pt + 40);
    SBAR();
    asm volatile("s_waitcnt vmcnt(7)\n\ts_barrier" ::: "memory"); SBAR();
    acc2 = MFMA(xf[12], wf0, acc2);
    SBAR();
    rb2 += -98304; PIN(rb2); wf2 = ldb128(rb2 + 0);
    SBAR();
    vi0 = FMA(prm[1], hr1, vi0); PIN(vi0); SBAR(); vr0 = FMA(-prm[1], hi1, vr0); PIN(vr0); SBAR();
    SBAR();
    vr1 = acc0[9] * rt40[1]; PIN(vr1); SBAR(); vi1 = acc1[9] * rt40[1]; PIN(vi1); SBAR();
    SBAR();
    acc2 = MFMA(xf[13], wf1, acc2);
    SBAR();
    rb3 += -98304; PIN(rb3); wf3 = ldb128(rb3 + 0);
    SBAR();
    hr0 = FMA(prm[0], hr1, vr0); PIN(hr0); SBAR(); hi0 = FMA(prm[0], hi1, vi0); PIN(hi0); SBAR();
    SBAR();
    vi1 = FMA(prm[1], hr0, vi1); PIN(vi1); SBAR(); vr1 = FMA(-prm[1], hi0, vr1); PIN(vr1); SBAR();
    SBAR();
    vr0 = acc0[10] * rt41[0]; PIN(vr0); SBAR(); vi0 = acc1[10] * rt41[0]; PIN(vi0); SBAR();
    SBAR();
    rt40 = ldf2(pt + 48);
    SBAR();
    GL(gbi, kx2 + 73728, voffR, 98304);
    SBAR();
    acc2 = MFMA(xf[14], wf2, acc2);
    SBAR();
    wf4 = ldb128(rb0 + 8192);
    SBAR();
    hi1 = FMA(prm[0], hi0, vi1); PIN(hi1); SBAR(); hr1 = FMA(prm[0], hr0, vr1); PIN(hr1); SBAR();
    SBAR();
    hli[4] = cvtpk(hi0, hi1); PIN(hli[4]); SBAR(); hlr[4] = cvtpk(hr0, hr1); PIN(hlr[4]); SBAR();
    SBAR();
    vi0 = FMA(prm[1], hr1, vi0); PIN(vi0); SBAR(); vr0 = FMA(-prm[1], hi1, vr0); PIN(vr0); SBAR();
    SBAR();
    vr1 = acc0[11] * rt41[1]; PIN(vr1); SBAR(); vi1 = acc1[11] * rt41[1]; PIN(vi1); SBAR();
    SBAR();
    acc2 = MFMA(xf[15], wf3, acc2);
    SBAR();
    wf5 = ldb128(rb1 + 8192);
    SBAR();
    hr0 = FMA(prm[0], hr1, vr0); PIN(hr0); SBAR(); hi0 = FMA(prm[0], hi1, vi0); PIN(hi0); SBAR();
    SBAR();
    vi1 = FMA(prm[1], hr0, vi1); PIN(vi1); SBAR(); vr1 = FMA(-prm[1], hi0, vr1); PIN(vr1); SBAR();
    SBAR();
    vr0 = acc0[12] * rt40[0]; PIN(vr0); SBAR(); vi0 = acc1[12] * rt40[0]; PIN(vi0); SBAR();
    SBAR();
    rt41 = ldf2(pt + 56);
    SBAR();
    acc2 = MFMA(xf[16], wf4, acc2);
    SBAR();
    wf6 = ldb128(rb2 + 8192);
    SBAR();
    hi1 = FMA(prm[0], hi0, vi1); PIN(hi1); SBAR(); hr1 = FMA(prm[0], hr0, vr1); PIN(hr1); SBAR();
    SBAR();
    hli[5] = cvtpk(hi0, hi1); PIN(hli[5]); SBAR(); hlr[5] = cvtpk(hr0, hr1); PIN(hlr[5]); SBAR();
    SBAR();
    vi0 = FMA(prm[1], hr1, vi0); PIN(vi0); SBAR(); vr0 = FMA(-prm[1], hi1, vr0); PIN(vr0); SBAR();
    SBAR();
    vr1 = acc0[13] * rt40[1]; PIN(vr1); SBAR(); vi1 = acc1[13] * rt40[1]; PIN(vi1); SBAR();
    SBAR();
    acc2 = MFMA(xf[17], wf5, acc2);
    SBAR();
    wf7 = ldb128(rb3 + 8192);
    SBAR();
    hr0 = FMA(prm[0], hr1, vr0); PIN(hr0); SBAR(); hi0 = FMA(prm[0], hi1, vi0); PIN(hi0); SBAR();
    SBAR();
    vi1 = FMA(prm[1], hr0, vi1); PIN(vi1); SBAR(); vr1 = FMA(-prm[1], hi0, vr1); PIN(vr1); SBAR();
    SBAR();
    vr0 = acc0[14] * rt41[0]; PIN(vr0); SBAR(); vi0 = acc1[14] * rt41[0]; PIN(vi0); SBAR();
    SBAR();
    GL(gbi, kx2 + 73856, voffR, 106496);
    SBAR();
    acc2 = MFMA(xf[18], wf6, acc2);
    SBAR();
    wf8 = ldb128(rb0 + 16384);
    SBAR();
    hi1 = FMA(prm[0], hi0, vi1); PIN(hi1); SBAR(); hr1 = FMA(prm[0], hr0, vr1); PIN(hr1); SBAR();
    SBAR();
    hli[6] = cvtpk(hi0, hi1); PIN(hli[6]); SBAR(); hlr[6] = cvtpk(hr0, hr1); PIN(hlr[6]); SBAR();
    SBAR();
    vi0 = FMA(prm[1], hr1, vi0); PIN(vi0); SBAR(); vr0 = FMA(-prm[1], hi1, vr0); PIN(vr0); SBAR();
    SBAR();
    acc2 = MFMA(xf[19], wf7, acc2);
    SBAR();
    wf9 = ldb128(rb1 + 16384);
    SBAR();
    hr0 = FMA(prm[0], hr1, vr0); PIN(hr0); SBAR(); hi0 = FMA(prm[0], hi1, vi0); PIN(hi0); SBAR();
    SBAR();
    vr1 = acc0[15] * rt41[1]; PIN(vr1); SBAR(); vi1 = acc1[15] * rt41[1]; PIN(vi1); SBAR();
    SBAR();
    acc2 = MFMA(xf[20], wf8, acc2);
    SBAR();
    wf10 = ldb128(rb2 + 16384);
    SBAR();
    vi1 = FMA(prm[1], hr0, vi1); PIN(vi1); SBAR(); vr1 = FMA(-prm[1], hi0, vr1); PIN(vr1); SBAR();
    SBAR();
    acc2 = MFMA(xf[21], wf9, acc2);
    SBAR();
    wf11 = ldb128(rb3 + 16384);
    SBAR();
    hi1 = FMA(prm[0], hi0, vi1); PIN(hi1); SBAR(); hr1 = FMA(prm[0], hr0, vr1); PIN(hr1); SBAR();
    SBAR();
    hli[7] = cvtpk(hi0, hi1); PIN(hli[7]); SBAR(); hlr[7] = cvtpk(hr0, hr1); PIN(hlr[7]); SBAR();
    SBAR();
    stu4(HW(1) + 4096, (u32x4){hli[4], hli[5], hli[6], hli[7]});
    SBAR();
    stf2(pw + 32768, hr1, hi1);
    SBAR();
    GL(gbi, kx2 + 73984, voffR, 114688);
    SBAR();
    acc2 = MFMA(xf[22], wf10, acc2);
    SBAR();
    wf0 = ldb128(rb0 + 24576);
    wf1 = ldb128(rb1 + 24576);
    SBAR();
    acc2 = MFMA(xf[23], wf11, acc2);
    SBAR();
    asm volatile("s_waitcnt vmcnt(7) lgkmcnt(2)\n\ts_barrier" ::: "memory"); SBAR();
    acc2 = MFMA(xf[24], wf0, acc2);
    SBAR();
    wf2 = ldb128(rb2 + 24576);
    SBAR();
    ea0 = ldf2(sq + 32768); eb0 = ldf2(sq + 33280);
    SBAR();
    acc2 = MFMA(xf[25], wf1, acc2);
    SBAR();
    wf3 = ldb128(rb3 + 24576);
    SBAR();
    ea1 = ldf2(sq + 33792); eb1 = ldf2(sq + 34304);
    SBAR();
    GL(gbr, kx0 + 147456, voffR, 0);
    SBAR();
    acc2 = MFMA(xf[26], wf2, acc2);
    SBAR();
    wf4 = ldb128(rb0 + 32768);
    SBAR();
    VWAIT1(5, tb0);
    SBAR();
    Pr = ea0.x; Pi = ea0.y; Pwr = seg > 0 ? Pr : 0.f; PIN(Pwr); SBAR(); Pwi = seg > 0 ? Pi : 0.f; PIN(Pwi); SBAR();
    SBAR();
    acc2 = MFMA(xf[27], wf3, acc2);
    SBAR();
    wf5 = ldb128(rb1 + 32768);
    SBAR();
    t0_ = FMA(-TBF(tb0, 1), Pi, eb0.x); PIN(t0_); SBAR(); t1_ = FMA(TBF(tb0, 1), Pr, eb0.y); PIN(t1_); SBAR(); Pr = FMA(TBF(tb0, 0), Pr, t0_); PIN(Pr); SBAR(); Pi = FMA(TBF(tb0, 0), Pi, t1_); PIN(Pi); SBAR(); Pwr = seg > 1 ? Pr : Pwr; PIN(Pwr); SBAR(); Pwi = seg > 1 ? Pi : Pwi; PIN(Pwi); SBAR();
    SBAR();
    acc2 = MFMA(xf[28], wf4, acc2);
    SBAR();
    wf6 = ldb128(rb2 + 32768);
    SBAR();
    ea0 = ldf2(sq + 34816); eb0 = ldf2(sq + 35328);
    SBAR();
    t0_ = FMA(-TBF(tb0, 1), Pi, ea1.x); PIN(t0_); SBAR(); t1_ = FMA(TBF(tb0, 1), Pr, ea1.y); PIN(t1_); SBAR(); Pr = FMA(TBF(tb0, 0), Pr, t0_); PIN(Pr); SBAR(); Pi = FMA(TBF(tb0, 0), Pi, t1_); PIN(Pi); SBAR(); Pwr = seg > 2 ? Pr : Pwr; PIN(Pwr); SBAR(); Pwi = seg > 2 ? Pi : Pwi; PIN(Pwi); SBAR();
    SBAR();
    acc2 = MFMA(xf[29], wf5, acc2);
    SBAR();
    wf7 = ldb128(rb3 + 32768);
    SBAR();
    t0_ = FMA(-TBF(tb0, 1), Pi, eb1.x); PIN(t0_); SBAR(); t1_ = FMA(TBF(tb0, 1), Pr, eb1.y); PIN(t1_); SBAR(); Pr = FMA(TBF(tb0, 0), Pr, t0_); PIN(Pr); SBAR(); Pi = FMA(TBF(tb0, 0), Pi, t1_); PIN(Pi); SBAR(); Pwr = seg > 3 ? Pr : Pwr; PIN(Pwr); SBAR(); Pwi = seg > 3 ? Pi : Pwi; PIN(Pwi); SBAR();
    SBAR();
    GL(gbr, kx0 + 147584, voffR, 8192);
    SBAR();
    acc2 = MFMA(xf[30], wf6, acc2);
    SBAR();
    wf8 = ldb128(rb0 + 40960);
    SBAR();
    ea1 = ldf2(sq + 35840); eb1 = ldf2(sq + 36352);
    SBAR();
    t0_ = FMA(-TBF(tb0, 1), Pi, ea0.x); PIN(t0_); SBAR(); t1_ = FMA(TBF(tb0, 1), Pr, ea0.y); PIN(t1_); SBAR(); Pr = FMA(TBF(tb0, 0), Pr, t0_); PIN(Pr); SBAR(); Pi = FMA(TBF(tb0, 0), Pi, t1_); PIN(Pi); SBAR(); Pwr = seg > 4 ? Pr : Pwr; PIN(Pwr); SBAR(); Pwi = seg > 4 ? Pi : Pwi; PIN(Pwi); SBAR();
    SBAR();
    acc2 = MFMA(xf[31], wf7, acc2);
    SBAR();
    wf9 = ldb128(rb1 + 40960);
    SBAR();
    t0_ = FMA(-TBF(tb0, 1), Pi, eb0.x); PIN(t0_); SBAR(); t1_ = FMA(TBF(tb0, 1), Pr, eb0.y); PIN(t1_); SBAR(); Pr = FMA(TBF(tb0, 0), Pr, t0_); PIN(Pr); SBAR(); Pi = FMA(TBF(tb0, 0), Pi, t1_); PIN(Pi); SBAR(); Pwr = seg > 5 ? Pr : Pwr; PIN(Pwr); SBAR(); Pwi = seg > 5 ? Pi : Pwi; PIN(Pwi); SBAR();
    SBAR();
    acc2 = MFMA(xf[32], wf8, acc2);
    SBAR();
    wf10 = ldb128(rb2 + 40960);
    SBAR();
    t0_ = FMA(-TBF(tb0, 1), Pi, ea1.x); PIN(t0_); SBAR(); t1_ = FMA(TBF(tb0, 1), Pr, ea1.y); PIN(t1_); SBAR(); Pr = FMA(TBF(tb0, 0), Pr, t0_); PIN(Pr); SBAR(); Pi = FMA(TBF(tb0, 0), Pi, t1_); PIN(Pi); SBAR(); Pwr = seg > 6 ? Pr : Pwr; PIN(Pwr); SBAR(); Pwi = seg > 6 ? Pi : Pwi; PIN(Pwi); SBAR();
    SBAR();
    acc2 = MFMA(xf[33], wf9, acc2);
    SBAR();
    wf11 = ldb128(rb3 + 40960);
    SBAR();
    t0_ = FMA(-TBF(tb0, 1), Pi, eb1.x); PIN(t0_); SBAR(); t1_ = FMA(TBF(tb0, 1), Pr, eb1.y); PIN(t1_); SBAR(); Pr = FMA(TBF(tb0, 0), Pr, t0_); PIN(Pr); SBAR(); Pi = FMA(TBF(tb0, 0), Pi, t1_); PIN(Pi); SBAR();
    SBAR();
    GL(gbr, kx0 + 147712, voffR, 16384);
    SBAR();
    acc2 = MFMA(xf[34], wf10, acc2);
    SBAR();
    rb0 += 49152; PIN(rb0); wf0 = ldb128(rb0 + 0);
    rb1 += 49152; PIN(rb1); wf1 = ldb128(rb1 + 0);
    SBAR();
    PUBLISH(0);
    SBAR();
    acc2 = MFMA(xf[35], wf11, acc2);
    SBAR();
    asm volatile("s_waitcnt vmcnt(6)\n\ts_barrier" ::: "memory"); SBAR();
    acc0 = MFMA(xf[0], wf0, zero16);
    SBAR();
    rb2 += 49152; PIN(rb2); wf2 = ldb128(rb2 + 0);
    SBAR();
    acc0 = MFMA(xf[1], wf1, acc0);
    SBAR();
    rb3 += 49152; PIN(rb3); wf3 = ldb128(rb3 + 0);
    SBAR();
    GL(gbr, kx1 + 147456, voffR, 24576);
    SBAR();
    acc0 = MFMA(xf[2], wf2, acc0);
    SBAR();
    wf4 = ldb128(rb0 + 8192);
    SBAR();
    acc0 = MFMA(xf[3], wf3, acc0);
    SBAR();
    wf5 = ldb128(rb1 + 8192);
    SBAR();
    acc0 = MFMA(xf[4], wf4, acc0);
    SBAR();
    wf6 = ldb128(rb2 + 8192);
    SBAR();
    acc0 = MFMA(xf[5], wf5, acc0);
    SBAR();
    wf7 = ldb128(rb3 + 8192);
    SBAR();
    GL(gbr, kx1 + 147584, voffR, 32768);
    SBAR();
    acc0 = MFMA(xf[6], wf6, acc0);
    SBAR();
    wf8 = ldb128(rb0 + 16384);
    SBAR();
    acc0 = MFMA(xf[7], wf7, acc0);
    SBAR();
    wf9 = ldb128(rb1 + 16384);
    SBAR();
    acc0 = MFMA(xf[8], wf8, acc0);
    SBAR();
    wf10 = ldb128(rb2 + 16384);
    SBAR();
    acc0 = MFMA(xf[9], wf9, acc0);
    SBAR();
    wf11 = ldb128(rb3 + 16384);
    SBAR();
    GL(gbr, kx1 + 147712, voffR, 40960);
    SBAR();
    acc0 = MFMA(xf[10], wf10, acc0);
    SBAR();
    wf0 = ldb128(rb0 + 24576);
    wf1 = ldb128(rb1 + 24576);
    SBAR();
    ALD4(cor2, voffC, gco + 2048);
    SBAR();
    acc0 = MFMA(xf[11], wf11, acc0);
    SBAR();
    ALD4(cor, voffC, gco + 3072);
    SBAR();
    asm volatile("s_waitcnt vmcnt(8)\n\ts_barrier" ::: "memory"); SBAR();
    acc0 = MFMA(xf[12], wf0, acc0);
    SBAR();
    wf2 = ldb128(rb2 + 24576);
    SBAR();
    acc0 = MFMA(xf[13], wf1, acc0);
    SBAR();
    wf3 = ldb128(rb3 + 24576);
    SBAR();
    GL(gbr, kx2 + 147456, voffR, 49152);
    SBAR();
    acc0 = MFMA(xf[14], wf2, acc0);
    SBAR();
    wf4 = ldb128(rb0 + 32768);
    SBAR();
    acc0 = MFMA(xf[15], wf3, acc0);
    SBAR();
    wf5 = ldb128(rb1 + 32768);
    SBAR();
    acc0 = MFMA(xf[16], wf4, acc0);
    SBAR();
    wf6 = ldb128(rb2 + 32768);
    SBAR();
    acc0 = MFMA(xf[17], wf5, acc0);
    SBAR();
    wf7 = ldb128(rb3 + 32768);
    SBAR();
    GL(gbr, kx2 + 147584, voffR, 57344);
    SBAR();
    acc0 = MFMA(xf[18], wf6, acc0);
    SBAR();
    wf8 = ldb128(rb0 + 40960);
    SBAR();
    acc0 = MFMA(xf[19], wf7, acc0);
    SBAR();
    wf9 = ldb128(rb1 + 40960);
    SBAR();
    acc0 = MFMA(xf[20], wf8, acc0);
    SBAR();
    wf10 = ldb128(rb2 + 40960);
    SBAR();
    acc0 = MFMA(xf[21], wf9, acc0);
    SBAR();
    wf11 = ldb128(rb3 + 40960);
    SBAR();
    GL(gbr, kx2 + 147712, voffR, 65536);
    SBAR();
    acc0 = MFMA(xf[22], wf10, acc0);
    SBAR();
    rb0 += 49152; PIN(rb0); wf0 = ldb128(rb0 + 0);
    rb1 += 49152; PIN(rb1); wf1 = ldb128(rb1 + 0);
    SBAR();
    acc0 = MFMA(xf[23], wf11, acc0);
    SBAR();
    asm volatile("s_waitcnt vmcnt(8)\n\ts_barrier" ::: "memory"); SBAR();
    acc0 = MFMA(xf[24], wf0, acc0);
    SBAR();
    rb2 += 49152; PIN(rb2); wf2 = ldb128(rb2 + 0);
    SBAR();
    LB_ADDR(); ALD4(g0, lbo0, ggr + 0); ALD4(g1, lbo1, ggr + 0); ALD4(tc, voffC, gpr + 6144);
    SBAR();
    acc0 = MFMA(xf[25], wf1, acc0);
    SBAR();
    rb3 += 49152; PIN(rb3); wf3 = ldb128(rb3 + 0);
    SBAR();
    GL(gbi, kx0 + 147456, voffR, 73728);
    SBAR();
    acc0 = MFMA(xf[26], wf2, acc0);
    SBAR();
    wf4 = ldb128(rb0 + 8192);
    SBAR();
    acc0 = MFMA(xf[27], wf3, acc0);
    SBAR();
    wf5 = ldb128(rb1 + 8192);
    SBAR();
    acc0 = MFMA(xf[28], wf4, acc0);
    SBAR();
    wf6 = ldb128(rb2 + 8192);
    SBAR();
    acc0 = MFMA(xf[29], wf5, acc0);
    SBAR();
    wf7 = ldb128(rb3 + 8192);
    SBAR();
    GL(gbi, kx0 + 147584, voffR, 81920);
    SBAR();
    acc0 = MFMA(xf[30], wf6, acc0);
    SBAR();
    wf8 = ldb128(rb0 + 16384);
    SBAR();
    VWAIT1(9, cor2); CHK_COR(cor2, 2);
    SBAR();
    acc0 = MFMA(xf[31], wf7, acc0);
    SBAR();
    wf9 = ldb128(rb1 + 16384);
    SBAR();
    VWAIT1(8, cor); CHK_COR(cor, 3);
    SBAR();
    acc0 = MFMA(xf[32], wf8, acc0);
    SBAR();
    wf10 = ldb128(rb2 + 16384);
    SBAR();
    prm = ldf2(sq + 37888); MK_PT();
    SBAR();
    acc2 = MFMA(lnf, CORF2, acc2);
    SBAR();
    acc0 = MFMA(xf[33], wf9, acc0);
    SBAR();
    wf11 = ldb128(rb3 + 16384);
    SBAR();
    GL(gbi, kx0 + 147712, voffR, 90112);
    SBAR();
    acc0 = MFMA(lnf, CORF, acc0);
    SBAR();
    acc0 = MFMA(xf[34], wf10, acc0);
    SBAR();
    rb0 += -98304; PIN(rb0); wf0 = ldb128(rb0 + 0);
    rb1 += -98304; PIN(rb1); wf1 = ldb128(rb1 + 0);
    SBAR();
    acc0 = MFMA(xf[35], wf11, acc0);
    SBAR();
    rt40 = ldf2(pt + 0);
    SBAR();
    asm volatile("s_waitcnt vmcnt(11)\n\ts_barrier" ::: "memory"); SBAR();
    acc1 = MFMA(xf[0], wf0, zero16);
    SBAR();
    rb2 += -98304; PIN(rb2); wf2 = ldb128(rb2 + 0);
    SBAR();
    acc1 = MFMA(xf[1], wf1, acc1);
    SBAR();
    rb3 += -98304; PIN(rb3); wf3 = ldb128(rb3 + 0);
    SBAR();
    VWAIT3(3, g0, g1, tc); LB_CHECK(0);
    SBAR();
    hr0 = acc2[0] * rt40[0]; PIN(hr0); SBAR(); hi0 = acc0[0] * rt40[0]; PIN(hi0); SBAR();
    SBAR();
    rt41 = ldf2(pt + 8);
    SBAR();
    GL(gbi, kx1 + 147456, voffR, 98304);
    SBAR();
    acc1 = MFMA(xf[2], wf2, acc1);
    SBAR();
    wf4 = ldb128(rb0 + 8192);
    SBAR();
    t0_ = FMA(-TBF(tc, 1), TBF(g1, 2), TBF(g0, 0)); PIN(t0_); SBAR(); t1_ = FMA(TBF(tc, 1), TBF(g1, 0), TBF(g0, 2)); PIN(t1_); SBAR(); t0_ = FMA(TBF(tc, 0), TBF(g1, 0), t0_); PIN(t0_); SBAR(); t1_ = FMA(TBF(tc, 0), TBF(g1, 2), t1_); PIN(t1_); SBAR();
    SBAR();
    vr1 = acc2[1] * rt40[1]; PIN(vr1); SBAR(); vi1 = acc0[1] * rt40[1]; PIN(vi1); SBAR();
    SBAR();
    acc1 = MFMA(xf[3], wf3, acc1);
    SBAR();
    wf5 = ldb128(rb1 + 8192);
    SBAR();
    stf2(pw, t0_, t1_);
    SBAR();
    vi1 = FMA(prm[1], hr0, vi1); PIN(vi1); SBAR(); vr1 = FMA(-prm[1], hi0, vr1); PIN(vr1); SBAR();
    SBAR();
    vr0 = acc2[2] * rt41[0]; PIN(vr0); SBAR(); vi0 = acc0[2] * rt41[0]; PIN(vi0); SBAR();
    SBAR();
    rt40 = ldf2(pt + 16);
    SBAR();
    acc1 = MFMA(xf[4], wf4, acc1);
    SBAR();
    wf6 = ldb128(rb2 + 8192);
    SBAR();
    hi1 = FMA(prm[0], hi0, vi1); PIN(hi1); SBAR(); hr1 = FMA(prm[0], hr0, vr1); PIN(hr1); SBAR();
    SBAR();
    hli[8] = cvtpk(hi0, hi1); PIN(hli[8]); SBAR(); hlr[8] = cvtpk(hr0, hr1); PIN(hlr[8]); SBAR();
    SBAR();
    vi0 = FMA(prm[1], hr1, vi0); PIN(vi0); SBAR(); vr0 = FMA(-prm[1], hi1, vr0); PIN(vr0); SBAR();
    SBAR();
    vr1 = acc2[3] * rt41[1]; PIN(vr1); SBAR(); vi1 = acc0[3] * rt41[1]; PIN(vi1); SBAR();
    SBAR();
    acc1 = MFMA(xf[5], wf5, acc1);
    SBAR();
    wf7 = ldb128(rb3 + 8192);
    SBAR();
    hr0 = FMA(prm[0], hr1, vr0); PIN(hr0); SBAR(); hi0 = FMA(prm[0], hi1, vi0); PIN(hi0); SBAR();
    SBAR();
    vi1 = FMA(prm[1], hr0, vi1); PIN(vi1); SBAR(); vr1 = FMA(-prm[1], hi0, vr1); PIN(vr1); SBAR();
    SBAR();
    vr0 = acc2[4] * rt40[0]; PIN(vr0); SBAR(); vi0 = acc0[4] * rt40[0]; PIN(vi0); SBAR();
    SBAR();
    GL(gbi, kx1 + 147584, voffR, 106496);
    SBAR();
    acc1 = MFMA(xf[6], wf6, acc1);
    SBAR();
    wf8 = ldb128(rb0 + 16384);
    SBAR();
    hi1 = FMA(prm[0], hi0, vi1); PIN(hi1); SBAR(); hr1 = FMA(prm[0], hr0, vr1); PIN(hr1); SBAR();
    SBAR();
    hli[9] = cvtpk(hi0, hi1); PIN(hli[9]); SBAR(); hlr[9] = cvtpk(hr0, hr1); PIN(hlr[9]); SBAR();
    SBAR();
    vi0 = FMA(prm[1], hr1, vi0); PIN(vi0); SBAR(); vr0 = FMA(-prm[1], hi1, vr0); PIN(vr0); SBAR();
    SBAR();
    rt41 = ldf2(pt + 24);
    SBAR();
    acc1 = MFMA(xf[7], wf7, acc1);
    SBAR();
    wf9 = ldb128(rb1 + 16384);
    SBAR();
    hr0 = FMA(prm[0], hr1, vr0); PIN(hr0); SBAR(); hi0 = FMA(prm[0], hi1, vi0); PIN(hi0); SBAR();
    SBAR();
    vr1 = acc2[5] * rt40[1]; PIN(vr1); SBAR(); vi1 = acc0[5] * rt40[1]; PIN(vi1); SBAR();
    SBAR();
    acc1 = MFMA(xf[8], wf8, acc1);
    SBAR();
    wf10 = ldb128(rb2 + 16384);
    SBAR();
    MK_S1(); ALD4(tb1, voffS1, gpr + 10240);
    SBAR();
    vi1 = FMA(prm[1], hr0, vi1); PIN(vi1); SBAR(); vr1 = FMA(-prm[1], hi0, vr1); PIN(vr1); SBAR();
    SBAR();
    vr0 = acc2[6] * rt41[0]; PIN(vr0); SBAR(); vi0 = acc0[6] * rt41[0]; PIN(vi0); SBAR();
    SBAR();
    rt40 = ldf2(pt + 32);
    SBAR();
    acc1 = MFMA(xf[9], wf9, acc1);
    SBAR();
    wf11 = ldb128(rb3 + 16384);
    SBAR();
    hi1 = FMA(prm[0], hi0, vi1); PIN(hi1); SBAR(); hr1 = FMA(prm[0], hr0, vr1); PIN(hr1); SBAR();
    SBAR();
    hli[10] = cvtpk(hi0, hi1); PIN(hli[10]); SBAR(); hlr[10] = cvtpk(hr0, hr1); PIN(hlr[10]); SBAR();
    SBAR();
    vi0 = FMA(prm[1], hr1, vi0); PIN(vi0); SBAR(); vr0 = FMA(-prm[1], hi1, vr0); PIN(vr0); SBAR();
    SBAR();
    vr1 = acc2[7] * rt41[1]; PIN(vr1); SBAR(); vi1 = acc0[7] * rt41[1]; PIN(vi1); SBAR();
    SBAR();
    GL(gbi, kx1 + 147712, voffR, 114688);
    SBAR();
    acc1 = MFMA(xf[10], wf10, acc1);
    SBAR();
    wf0 = ldb128(rb0 + 24576);
    wf1 = ldb128(rb1 + 24576);
    SBAR();
    hr0 = FMA(prm[0], hr1, vr0); PIN(hr0); SBAR(); hi0 = FMA(prm[0], hi1, vi0); PIN(hi0); SBAR();
    SBAR();
    vi1 = FMA(prm[1], hr0, vi1); PIN(vi1); SBAR(); vr1 = FMA(-prm[1], hi0, vr1); PIN(vr1); SBAR();
    SBAR();
    vr0 = acc2[8] * rt40[0]; PIN(vr0); SBAR(); vi0 = acc0[8] * rt40[0]; PIN(vi0); SBAR();
    SBAR();
    acc1 = MFMA(xf[11], wf11, acc1);
    SBAR();
    hi1 = FMA(prm[0], hi0, vi1); PIN(hi1); SBAR(); hr1 = FMA(prm[0], hr0, vr1); PIN(hr1); SBAR();
    SBAR();
    hli[11] = cvtpk(hi0, hi1); PIN(hli[11]); SBAR(); hlr[11] = cvtpk(hr0, hr1); PIN(hlr[11]); SBAR();
    SBAR();
    vi0 = FMA(prm[1], hr1, vi0); PIN(vi0); SBAR(); vr0 = FMA(-prm[1], hi1, vr0); PIN(vr0); SBAR();
    SBAR();
    vr1 = acc2[9] * rt40[1]; PIN(vr1); SBAR(); vi1 = acc0[9] * rt40[1]; PIN(vi1); SBAR();
    SBAR();
    rt41 = ldf2(pt + 40);
    SBAR();
    asm volatile("s_waitcnt vmcnt(10) lgkmcnt(2)\n\ts_barrier" ::: "memory"); SBAR();
    acc1 = MFMA(xf[12], wf0, acc1);
    SBAR();
    wf2 = ldb128(rb2 + 24576);
    SBAR();
    fa0 = ldf2(sq + 3584); fb0 = ldf2(sq + 3072);
    SBAR();
    hr0 = FMA(prm[0], hr1, vr0); PIN(hr0); SBAR(); hi0 = FMA(prm[0], hi1, vi0); PIN(hi0); SBAR();
    SBAR();
    vi1 = FMA(prm[1], hr0, vi1); PIN(vi1); SBAR(); vr1 = FMA(-prm[1], hi0, vr1); PIN(vr1); SBAR();
    SBAR();
    acc1 = MFMA(xf[13], wf1, acc1);
    SBAR();
    wf3 = ldb128(rb3 + 24576);
    SBAR();
    fa1 = ldf2(sq + 2560); fb1 = ldf2(sq + 2048);
    SBAR();
    hi1 = FMA(prm[0], hi0, vi1); PIN(hi1); SBAR(); hr1 = FMA(prm[0], hr0, vr1); PIN(hr1); SBAR();
    SBAR();
    hli[12] = cvtpk(hi0, hi1); PIN(hli[12]); SBAR(); hlr[12] = cvtpk(hr0, hr1); PIN(hlr[12]); SBAR();
    SBAR();
    vr0 = acc2[10] * rt41[0]; PIN(vr0); SBAR(); vi0 = acc0[10] * rt41[0]; PIN(vi0); SBAR();
    SBAR();
    rt40 = ldf2(pt + 48);
    SBAR();
    GL(gbi, kx2 + 147456, voffR, 0);
    SBAR();
    acc1 = MFMA(xf[14], wf2, acc1);
    SBAR();
    wf4 = ldb128(rb0 + 32768);
    SBAR();
    Cr = fa0.x; Ci = fa0.y;
    SBAR();
    vi0 = FMA(prm[1], hr1, vi0); PIN(vi0); SBAR(); vr0 = FMA(-prm[1], hi1, vr0); PIN(vr0); SBAR();
    SBAR();
    vr1 = acc2[11] * rt41[1]; PIN(vr1); SBAR(); vi1 = acc0[11] * rt41[1]; PIN(vi1); SBAR();
    SBAR();
    acc1 = MFMA(xf[15], wf3, acc1);
    SBAR();
    wf5 = ldb128(rb1 + 32768);
    SBAR();
    t2_ = FMA(-TBF(tc, 3), Ci, fb0.x); PIN(t2_); SBAR(); t3_ = FMA(TBF(tc, 3), Cr, fb0.y); PIN(t3_); SBAR(); Cr = FMA(TBF(tc, 2), Cr, t2_); PIN(Cr); SBAR(); Ci = FMA(TBF(tc, 2), Ci, t3_); PIN(Ci); SBAR();
    SBAR();
    hr0 = FMA(prm[0], hr1, vr0); PIN(hr0); SBAR(); hi0 = FMA(prm[0], hi1, vi0); PIN(hi0); SBAR();
    SBAR();
    vi1 = FMA(prm[1], hr0, vi1); PIN(vi1); SBAR(); vr1 = FMA(-prm[1], hi0, vr1); PIN(vr1); SBAR();
    SBAR();
    vr0 = acc2[12] * rt40[0]; PIN(vr0); SBAR(); vi0 = acc0[12] * rt40[0]; PIN(vi0); SBAR();
    SBAR();
    rt41 = ldf2(pt + 56);
    SBAR();
    acc1 = MFMA(xf[16], wf4, acc1);
    SBAR();
    wf6 = ldb128(rb2 + 32768);
    SBAR();
    t2_ = FMA(-TBF(tc, 3), Ci, fa1.x); PIN(t2_); SBAR(); t3_ = FMA(TBF(tc, 3), Cr, fa1.y); PIN(t3_); SBAR(); Cr = FMA(TBF(tc, 2), Cr, t2_); PIN(Cr); SBAR(); Ci = FMA(TBF(tc, 2), Ci, t3_); PIN(Ci); SBAR();
    SBAR();
    fa0 = ldf2(sq + 1536); fb0 = ldf2(sq + 1024);
    SBAR();
    hi1 = FMA(prm[0], hi0, vi1); PIN(hi1); SBAR(); hr1 = FMA(prm[0], hr0, vr1); PIN(hr1); SBAR();
    SBAR();
    hli[13] = cvtpk(hi0, hi1); PIN(hli[13]); SBAR(); hlr[13] = cvtpk(hr0, hr1); PIN(hlr[13]); SBAR();
    SBAR();
    vi0 = FMA(prm[1], hr1, vi0); PIN(vi0); SBAR(); vr0 = FMA(-prm[1], hi1, vr0); PIN(vr0); SBAR();
    SBAR();
    vr1 = acc2[13] * rt40[1]; PIN(vr1); SBAR(); vi1 = acc0[13] * rt40[1]; PIN(vi1); SBAR();
    SBAR();
    acc1 = MFMA(xf[17], wf5, acc1);
    SBAR();
    wf7 = ldb128(rb3 + 32768);
    SBAR();
    t2_ = FMA(-TBF(tc, 3), Ci, fb1.x); PIN(t2_); SBAR(); t3_ = FMA(TBF(tc, 3), Cr, fb1.y); PIN(t3_); SBAR(); Cr = FMA(TBF(tc, 2), Cr, t2_); PIN(Cr); SBAR(); Ci = FMA(TBF(tc, 2), Ci, t3_); PIN(Ci); SBAR();
    SBAR();
    hr0 = FMA(prm[0], hr1, vr0); PIN(hr0); SBAR(); hi0 = FMA(prm[0], hi1, vi0); PIN(hi0); SBAR();
    SBAR();
    vi1 = FMA(prm[1], hr0, vi1); PIN(vi1); SBAR(); vr1 = FMA(-prm[1], hi0, vr1); PIN(vr1); SBAR();
    SBAR();
    vr0 = acc2[14] * rt41[0]; PIN(vr0); SBAR(); vi0 = acc0[14] * rt41[0]; PIN(vi0); SBAR();
    SBAR();
    GL(gbi, kx2 + 147584, voffR, 8192);
    SBAR();
    acc1 = MFMA(xf[18], wf6, acc1);
    SBAR();
    wf8 = ldb128(rb0 + 40960);
    SBAR();
    t2_ = FMA(-TBF(tc, 3), Ci, fa0.x); PIN(t2_); SBAR(); t3_ = FMA(TBF(tc, 3), Cr, fa0.y); PIN(t3_); SBAR(); Cr = FMA(TBF(tc, 2), Cr, t2_); PIN(Cr); SBAR(); Ci = FMA(TBF(tc, 2), Ci, t3_); PIN(Ci); SBAR();
    SBAR();
    fa1 = ldf2(sq + 512); fb1 = ldf2(sq + 0);
    SBAR();
    hi1 = FMA(prm[0], hi0, vi1); PIN(hi1); SBAR(); hr1 = FMA(prm[0], hr0, vr1); PIN(hr1); SBAR();
    SBAR();
    hli[14] = cvtpk(hi0, hi1); PIN(hli[14]); SBAR(); hlr[14] = cvtpk(hr0, hr1); PIN(hlr[14]); SBAR();
    SBAR();
    vi0 = FMA(prm[1], hr1, vi0); PIN(vi0); SBAR(); vr0 = FMA(-prm[1], hi1, vr0); PIN(vr0); SBAR();
    SBAR();
    acc1 = MFMA(xf[19], wf7, acc1);
    SBAR();
    wf9 = ldb128(rb1 + 40960);
    SBAR();
    t2_ = FMA(-TBF(tc, 3), Ci, fb0.x); PIN(t2_); SBAR(); t3_ = FMA(TBF(tc, 3), Cr, fb0.y); PIN(t3_); SBAR(); Cr = FMA(TBF(tc, 2), Cr, t2_); PIN(Cr); SBAR(); Ci = FMA(TBF(tc, 2), Ci, t3_); PIN(Ci); SBAR();
    SBAR();
    hr0 = FMA(prm[0], hr1, vr0); PIN(hr0); SBAR(); hi0 = FMA(prm[0], hi1, vi0); PIN(hi0); SBAR();
    SBAR();
    vr1 = acc2[15] * rt41[1]; PIN(vr1); SBAR(); vi1 = acc0[15] * rt41[1]; PIN(vi1); SBAR();
    SBAR();
    acc1 = MFMA(xf[20], wf8, acc1);
    SBAR();
    wf10 = ldb128(rb2 + 40960);
    SBAR();
    t2_ = FMA(-TBF(tc, 3), Ci, fa1.x); PIN(t2_); SBAR(); t3_ = FMA(TBF(tc, 3), Cr, fa1.y); PIN(t3_); SBAR(); Cr = FMA(TBF(tc, 2), Cr, t2_); PIN(Cr); SBAR(); Ci = FMA(TBF(tc, 2), Ci, t3_); PIN(Ci); SBAR();
    SBAR();
    pfl = ldf2(sq + 37376); pfg = ldf2(sq + 38912);
    SBAR();
    vi1 = FMA(prm[1], hr0, vi1); PIN(vi1); SBAR(); vr1 = FMA(-prm[1], hi0, vr1); PIN(vr1); SBAR();
    SBAR();
    acc1 = MFMA(xf[21], wf9, acc1);
    SBAR();
    wf11 = ldb128(rb3 + 40960);
    SBAR();
    t2_ = FMA(-TBF(tc, 3), Ci, fb1.x); PIN(t2_); SBAR(); t3_ = FMA(TBF(tc, 3), Cr, fb1.y); PIN(t3_); SBAR(); Cr = FMA(TBF(tc, 2), Cr, t2_); PIN(Cr); SBAR(); Ci = FMA(TBF(tc, 2), Ci, t3_); PIN(Ci); SBAR();
    SBAR();
    hi1 = FMA(prm[0], hi0, vi1); PIN(hi1); SBAR(); hr1 = FMA(prm[0], hr0, vr1); PIN(hr1); SBAR();
    SBAR();
    hli[15] = cvtpk(hi0, hi1); PIN(hli[15]); SBAR(); hlr[15] = cvtpk(hr0, hr1); PIN(hlr[15]); SBAR();
    SBAR();
    stf2(pw + 32768, hr1, hi1);
    SBAR();
    GL(gbi, kx2 + 147712, voffR, 16384);
    SBAR();
    acc1 = MFMA(xf[22], wf10, acc1);
    SBAR();
    rb0 += 49152; PIN(rb0); wf0 = ldb128(rb0 + 0);
    rb1 += 49152; PIN(rb1); wf1 = ldb128(rb1 + 0);
    SBAR();
    t2_ = FMA(-TBF(tb0, 3), Ci, Pwr); PIN(t2_); SBAR(); t3_ = FMA(TBF(tb0, 3), Cr, Pwi); PIN(t3_); SBAR(); Cr = FMA(TBF(tb0, 2), Cr, t2_); PIN(Cr); SBAR(); Ci = FMA(TBF(tb0, 2), Ci, t3_); PIN(Ci); SBAR();
    SBAR();
    acc1 = MFMA(xf[23], wf11, acc1);
    SBAR();
    asm volatile("s_waitcnt vmcnt(7) lgkmcnt(2)\n\ts_barrier" ::: "memory"); SBAR();
    acc1 = MFMA(xf[24], wf0, acc1);
    SBAR();
    rb2 += 49152; PIN(rb2); wf2 = ldb128(rb2 + 0);
    SBAR();
    t2_ = pfg[1] * Ci; PIN(t2_); SBAR(); t3_ = pfg[1] * Cr; PIN(t3_); SBAR(); zr1 = FMA(pfg[0], Cr, -t2_); PIN(zr1); SBAR(); zi1 = FMA(pfg[0], Ci, t3_); PIN(zi1); SBAR();
    SBAR();
    { const u32x4 u_ = *(const LAS u32x4*)(HW(0) + 4096); hli[0] = u_[0]; hli[1] = u_[1]; hli[2] = u_[2]; hli[3] = u_[3]; }
    SBAR();
    ea0 = ldf2(sq + 32768); eb0 = ldf2(sq + 33280);
    SBAR();
    acc1 = MFMA(xf[25], wf1, acc1);
    SBAR();
    rb3 += 49152; PIN(rb3); wf3 = ldb128(rb3 + 0);
    SBAR();
    ea1 = ldf2(sq + 33792); eb1 = ldf2(sq + 34304);
    SBAR();
    GL(gcr, ROT9(0) * 73728 + 0, voffR, 24576);
    SBAR();
    acc1 = MFMA(xf[26], wf2, acc1);
    SBAR();
    wf4 = ldb128(rb0 + 8192);
    SBAR();
    t2_ = pfl[1] * zi1; PIN(t2_); SBAR(); t3_ = pfl[1] * zr1; PIN(t3_); SBAR(); ua = bflo(hlr[0]); PIN(ua); SBAR(); ub = bflo(hli[0]); PIN(ub); SBAR();
    SBAR();
    VWAIT1(5, tb1);
    SBAR();
    Pr = ea0.x; Pi = ea0.y; Pwr = seg > 0 ? Pr : 0.f; PIN(Pwr); SBAR(); Pwi = seg > 0 ? Pi : 0.f; PIN(Pwi); SBAR();
    SBAR();
    acc1 = MFMA(xf[27], wf3, acc1);
    SBAR();
    wf5 = ldb128(rb1 + 8192);
    SBAR();
    zi0 = FMA(pfl[0], zi1, t3_); PIN(zi0); SBAR(); zr0 = FMA(pfl[0], zr1, -t2_); PIN(zr0); SBAR();
    SBAR();
    t0_ = FMA(-TBF(tb1, 1), Pi, eb0.x); PIN(t0_); SBAR(); t1_ = FMA(TBF(tb1, 1), Pr, eb0.y); PIN(t1_); SBAR(); Pr = FMA(TBF(tb1, 0), Pr, t0_); PIN(Pr); SBAR(); Pi = FMA(TBF(tb1, 0), Pi, t1_); PIN(Pi); SBAR(); Pwr = seg > 1 ? Pr : Pwr; PIN(Pwr); SBAR(); Pwi = seg > 1 ? Pi : Pwi; PIN(Pwi); SBAR();
    SBAR();
    acc1 = MFMA(xf[28], wf4, acc1);
    SBAR();
    wf6 = ldb128(rb2 + 8192);
    SBAR();
    ui0 = FMA(-pfg[1], ua, -zi0); PIN(ui0); SBAR(); ur0 = FMA(-pfg[1], ub, zr0); PIN(ur0); SBAR();
    SBAR();
    ea0 = ldf2(sq + 34816); eb0 = ldf2(sq + 35328);
    SBAR();
    t0_ = FMA(-TBF(tb1, 1), Pi, ea1.x); PIN(t0_); SBAR(); t1_ = FMA(TBF(tb1, 1), Pr, ea1.y); PIN(t1_); SBAR(); Pr = FMA(TBF(tb1, 0), Pr, t0_); PIN(Pr); SBAR(); Pi = FMA(TBF(tb1, 0), Pi, t1_); PIN(Pi); SBAR(); Pwr = seg > 2 ? Pr : Pwr; PIN(Pwr); SBAR(); Pwi = seg > 2 ? Pi : Pwi; PIN(Pwi); SBAR();
    SBAR();
    acc1 = MFMA(xf[29], wf5, acc1);
    SBAR();
    wf7 = ldb128(rb3 + 8192);
    SBAR();
    ui0 = FMA(-pfg[0], ub, ui0); PIN(ui0); SBAR(); ur0 = FMA(pfg[0], ua, ur0); PIN(ur0); SBAR();
    SBAR();
    t0_ = FMA(-TBF(tb1, 1), Pi, eb1.x); PIN(t0_); SBAR(); t1_ = FMA(TBF(tb1, 1), Pr, eb1.y); PIN(t1_); SBAR(); Pr = FMA(TBF(tb1, 0), Pr, t0_); PIN(Pr); SBAR(); Pi = FMA(TBF(tb1, 0), Pi, t1_); PIN(Pi); SBAR(); Pwr = seg > 3 ? Pr : Pwr; PIN(Pwr); SBAR(); Pwi = seg > 3 ? Pi : Pwi; PIN(Pwi); SBAR();
    SBAR();
    GL(gci, ROT9(0) * 73728 + 0, voffR, 32768);
    SBAR();
    acc1 = MFMA(xf[30], wf6, acc1);
    SBAR();
    wf8 = ldb128(rb0 + 16384);
    SBAR();
    ea1 = ldf2(sq + 35840); eb1 = ldf2(sq + 36352);
    SBAR();
    t0_ = FMA(-TBF(tb1, 1), Pi, ea0.x); PIN(t0_); SBAR(); t1_ = FMA(TBF(tb1, 1), Pr, ea0.y); PIN(t1_); SBAR(); Pr = FMA(TBF(tb1, 0), Pr, t0_); PIN(Pr); SBAR(); Pi = FMA(TBF(tb1, 0), Pi, t1_); PIN(Pi); SBAR(); Pwr = seg > 4 ? Pr : Pwr; PIN(Pwr); SBAR(); Pwi = seg > 4 ? Pi : Pwi; PIN(Pwi); SBAR();
    SBAR();
    acc1 = MFMA(xf[31], wf7, acc1);
    SBAR();
    wf9 = ldb128(rb1 + 16384);
    SBAR();
    t2_ = pfl[1] * zi0; PIN(t2_); SBAR(); t3_ = pfl[1] * zr0; PIN(t3_); SBAR(); ua = bfhi(hlr[0]); PIN(ua); SBAR(); ub = bfhi(hli[0]); PIN(ub); SBAR();
    SBAR();
    t0_ = FMA(-TBF(tb1, 1), Pi, eb0.x); PIN(t0_); SBAR(); t1_ = FMA(TBF(tb1, 1), Pr, eb0.y); PIN(t1_); SBAR(); Pr = FMA(TBF(tb1, 0), Pr, t0_); PIN(Pr); SBAR(); Pi = FMA(TBF(tb1, 0), Pi, t1_); PIN(Pi); SBAR(); Pwr = seg > 5 ? Pr : Pwr; PIN(Pwr); SBAR(); Pwi = seg > 5 ? Pi : Pwi; PIN(Pwi); SBAR();
    SBAR();
    acc1 = MFMA(xf[32], wf8, acc1);
    SBAR();
    wf10 = ldb128(rb2 + 16384);
    SBAR();
    zi1 = FMA(pfl[0], zi0, t3_); PIN(zi1); SBAR(); zr1 = FMA(pfl[0], zr0, -t2_); PIN(zr1); SBAR();
    SBAR();
    t0_ = FMA(-TBF(tb1, 1), Pi, ea1.x); PIN(t0_); SBAR(); t1_ = FMA(TBF(tb1, 1), Pr, ea1.y); PIN(t1_); SBAR(); Pr = FMA(TBF(tb1, 0), Pr, t0_); PIN(Pr); SBAR(); Pi = FMA(TBF(tb1, 0), Pi, t1_); PIN(Pi); SBAR(); Pwr = seg > 6 ? Pr : Pwr; PIN(Pwr); SBAR(); Pwi = seg > 6 ? Pi : Pwi; PIN(Pwi); SBAR();
    SBAR();
    acc1 = MFMA(xf[33], wf9, acc1);
    SBAR();
    wf11 = ldb128(rb3 + 16384);
    SBAR();
    ui1 = FMA(-pfg[1], ua, -zi1); PIN(ui1); SBAR(); ur1 = FMA(-pfg[1], ub, zr1); PIN(ur1); SBAR();
    SBAR();
    t0_ = FMA(-TBF(tb1, 1), Pi, eb1.x); PIN(t0_); SBAR(); t1_ = FMA(TBF(tb1, 1), Pr, eb1.y); PIN(t1_); SBAR(); Pr = FMA(TBF(tb1, 0), Pr, t0_); PIN(Pr); SBAR(); Pi = FMA(TBF(tb1, 0), Pi, t1_); PIN(Pi); SBAR();
    SBAR();
    GL(gcr, ROT9(1) * 73728 + 0, voffR, 40960);
    SBAR();
    acc1 = MFMA(xf[34], wf10, acc1);
    SBAR();
    wf0 = ldb128(rb0 + 24576);
    wf1 = ldb128(rb1 + 24576);
    SBAR();
    ui1 = FMA(-pfg[0], ub, ui1); PIN(ui1); SBAR(); ur1 = FMA(pfg[0], ua, ur1); PIN(ur1); SBAR(); oi0 = cvtpk(ui0, ui1); PIN(oi0); SBAR(); or0 = cvtpk(ur0, ur1); PIN(or0); SBAR();
    SBAR();
    PUBLISH(1);
    SBAR();
    acc1 = MFMA(xf[35], wf11, acc1);
    SBAR();
    asm volatile("s_waitcnt vmcnt(6)\n\ts_barrier" ::: "memory"); SBAR();
    acc2 = MFMA(xf[0], wf0, zero16);
    SBAR();
    wf2 = ldb128(rb2 + 24576);
    SBAR();
    t2_ = pfl[1] * zi1; PIN(t2_); SBAR(); t3_ = pfl[1] * zr1; PIN(t3_); SBAR(); ua = bflo(hlr[1]); PIN(ua); SBAR(); ub = bflo(hli[1]); PIN(ub); SBAR();
    SBAR();
    zi0 = FMA(pfl[0], zi1, t3_); PIN(zi0); SBAR(); zr0 = FMA(pfl[0], zr1, -t2_); PIN(zr0); SBAR();
    SBAR();
    ui0 = FMA(-pfg[1], ua, -zi0); PIN(ui0); SBAR(); ur0 = FMA(-pfg[1], ub, zr0); PIN(ur0); SBAR();
    SBAR();
    acc2 = MFMA(xf[1], wf1, acc2);
    SBAR();
    wf3 = ldb128(rb3 + 24576);
    SBAR();
    ui0 = FMA(-pfg[0], ub, ui0); PIN(ui0); SBAR(); ur0 = FMA(pfg[0], ua, ur0); PIN(ur0); SBAR();
    SBAR();
    t2_ = pfl[1] * zi0; PIN(t2_); SBAR(); t3_ = pfl[1] * zr0; PIN(t3_); SBAR(); ua = bfhi(hlr[1]); PIN(ua); SBAR(); ub = bfhi(hli[1]); PIN(ub); SBAR();
    SBAR();
    zi1 = FMA(pfl[0], zi0, t3_); PIN(zi1); SBAR(); zr1 = FMA(pfl[0], zr0, -t2_); PIN(zr1); SBAR();
    SBAR();
    GL(gci, ROT9(1) * 73728 + 0, voffR, 49152);
    SBAR();
    acc2 = MFMA(xf[2], wf2, acc2);
    SBAR();
    wf4 = ldb128(rb0 + 32768);
    SBAR();
    ui1 = FMA(-pfg[1], ua, -zi1); PIN(ui1); SBAR(); ur1 = FMA(-pfg[1], ub, zr1); PIN(ur1); SBAR();
    SBAR();
    ui1 = FMA(-pfg[0], ub, ui1); PIN(ui1); SBAR(); ur1 = FMA(pfg[0], ua, ur1); PIN(ur1); SBAR(); oi1 = cvtpk(ui0, ui1); PIN(oi1); SBAR(); or1 = cvtpk(ur0, ur1); PIN(or1); SBAR();
    SBAR();
    acc2 = MFMA(xf[3], wf3, acc2);
    SBAR();
    wf5 = ldb128(rb1 + 32768);
    SBAR();
    t2_ = pfl[1] * zi1; PIN(t2_); SBAR(); t3_ = pfl[1] * zr1; PIN(t3_); SBAR(); ua = bflo(hlr[2]); PIN(ua); SBAR(); ub = bflo(hli[2]); PIN(ub); SBAR();
    SBAR();
    zi0 = FMA(pfl[0], zi1, t3_); PIN(zi0); SBAR(); zr0 = FMA(pfl[0], zr1, -t2_); PIN(zr0); SBAR();
    SBAR();
    ui0 = FMA(-pfg[1], ua, -zi0); PIN(ui0); SBAR(); ur0 = FMA(-pfg[1], ub, zr0); PIN(ur0); SBAR();
    SBAR();
    acc2 = MFMA(xf[4], wf4, acc2);
    SBAR();
    wf6 = ldb128(rb2 + 32768);
    SBAR();
    { const u32x4 u_ = *(const LAS u32x4*)(HW(1) + 4096); hli[4] = u_[0]; hli[5] = u_[1]; hli[6] = u_[2]; hli[7] = u_[3]; }
    SBAR();
    ui0 = FMA(-pfg[0], ub, ui0); PIN(ui0); SBAR(); ur0 = FMA(pfg[0], ua, ur0); PIN(ur0); SBAR();
    SBAR();
    t2_ = pfl[1] * zi0; PIN(t2_); SBAR(); t3_ = pfl[1] * zr0; PIN(t3_); SBAR(); ua = bfhi(hlr[2]); PIN(ua); SBAR(); ub = bfhi(hli[2]); PIN(ub); SBAR();
    SBAR();
    acc2 = MFMA(xf[5], wf5, acc2);
    SBAR();
    wf7 = ldb128(rb3 + 32768);
    SBAR();
    zi1 = FMA(pfl[0], zi0, t3_); PIN(zi1); SBAR(); zr1 = FMA(pfl[0], zr0, -t2_); PIN(zr1); SBAR();
    SBAR();
    ui1 = FMA(-pfg[1], ua, -zi1); PIN(ui1); SBAR(); ur1 = FMA(-pfg[1], ub, zr1); PIN(ur1); SBAR();
    SBAR();
    ui1 = FMA(-pfg[0], ub, ui1); PIN(ui1); SBAR(); ur1 = FMA(pfg[0], ua, ur1); PIN(ur1); SBAR(); oi2 = cvtpk(ui0, ui1); PIN(oi2); SBAR(); or2 = cvtpk(ur0, ur1); PIN(or2); SBAR();
    SBAR();
    GL(gcr, ROT9(2) * 73728 + 0, voffR, 57344);
    SBAR();
    acc2 = MFMA(xf[6], wf6, acc2);
    SBAR();
    wf8 = ldb128(rb0 + 40960);
    SBAR();
    t2_ = pfl[1] * zi1; PIN(t2_); SBAR(); t3_ = pfl[1] * zr1; PIN(t3_); SBAR(); ua = bflo(hlr[3]); PIN(ua); SBAR(); ub = bflo(hli[3]); PIN(ub); SBAR();
    SBAR();
    zi0 = FMA(pfl[0], zi1, t3_); PIN(zi0); SBAR(); zr0 = FMA(pfl[0], zr1, -t2_); PIN(zr0); SBAR();
    SBAR();
    acc2 = MFMA(xf[7], wf7, acc2);
    SBAR();
    wf9 = ldb128(rb1 + 40960);
    SBAR();
    ui0 = FMA(-pfg[1], ua, -zi0); PIN(ui0); SBAR(); ur0 = FMA(-pfg[1], ub, zr0); PIN(ur0); SBAR();
    SBAR();
    ui0 = FMA(-pfg[0], ub, ui0); PIN(ui0); SBAR(); ur0 = FMA(pfg[0], ua, ur0); PIN(ur0); SBAR();
    SBAR();
    t2_ = pfl[1] * zi0; PIN(t2_); SBAR(); t3_ = pfl[1] * zr0; PIN(t3_); SBAR(); ua = bfhi(hlr[3]); PIN(ua); SBAR(); ub = bfhi(hli[3]); PIN(ub); SBAR();
    SBAR();
    acc2 = MFMA(xf[8], wf8, acc2);
    SBAR();
    wf10 = ldb128(rb2 + 40960);
    SBAR();
    zi1 = FMA(pfl[0], zi0, t3_); PIN(zi1); SBAR(); zr1 = FMA(pfl[0], zr0, -t2_); PIN(zr1); SBAR();
    SBAR();
    ui1 = FMA(-pfg[1], ua, -zi1); PIN(ui1); SBAR(); ur1 = FMA(-pfg[1], ub, zr1); PIN(ur1); SBAR();
    SBAR();
    acc2 = MFMA(xf[9], wf9, acc2);
    SBAR();
    wf11 = ldb128(rb3 + 40960);
    SBAR();
    ui1 = FMA(-pfg[0], ub, ui1); PIN(ui1); SBAR(); ur1 = FMA(pfg[0], ua, ur1); PIN(ur1); SBAR(); oi3 = cvtpk(ui0, ui1); PIN(oi3); SBAR(); or3 = cvtpk(ur0, ur1); PIN(or3); SBAR(); { const lptr p_ = HW(0); stu4(p_ + 4096, (u32x4){oi0, oi1, oi2, oi3}); stu4(p_, (u32x4){or0, or1, or2, or3}); }
    SBAR();
    t2_ = pfl[1] * zi1; PIN(t2_); SBAR(); t3_ = pfl[1] * zr1; PIN(t3_); SBAR(); ua = bflo(hlr[4]); PIN(ua); SBAR(); ub = bflo(hli[4]); PIN(ub); SBAR();
    SBAR();
    zi0 = FMA(pfl[0], zi1, t3_); PIN(zi0); SBAR(); zr0 = FMA(pfl[0], zr1, -t2_); PIN(zr0); SBAR();
    SBAR();
    GL(gci, ROT9(2) * 73728 + 0, voffR, 65536);
    SBAR();
    acc2 = MFMA(xf[10], wf10, acc2);
    SBAR();
    rb0 += 49152; PIN(rb0); wf0 = ldb128(rb0 + 0);
    rb1 += 49152; PIN(rb1); wf1 = ldb128(rb1 + 0);
    SBAR();
    ALD4(cor2, voffC, gco + 4096);
    SBAR();
    ui0 = FMA(-pfg[1], ua, -zi0); PIN(ui0); SBAR(); ur0 = FMA(-pfg[1], ub, zr0); PIN(ur0); SBAR();
    SBAR();
    ui0 = FMA(-pfg[0], ub, ui0); PIN(ui0); SBAR(); ur0 = FMA(pfg[0], ua, ur0); PIN(ur0); SBAR();
    SBAR();
    acc2 = MFMA(xf[11], wf11, acc2);
    SBAR();
    ALD4(cor, voffC, gco + 5120);
    SBAR();
    t2_ = pfl[1] * zi0; PIN(t2_); SBAR(); t3_ = pfl[1] * zr0; PIN(t3_); SBAR(); ua = bfhi(hlr[4]); PIN(ua); SBAR(); ub = bfhi(hli[4]); PIN(ub); SBAR();
    SBAR();
    zi1 = FMA(pfl[0], zi0, t3_); PIN(zi1); SBAR(); zr1 = FMA(pfl[0], zr0, -t2_); PIN(zr1); SBAR();
    SBAR();
    ui1 = FMA(-pfg[1], ua, -zi1); PIN(ui1); SBAR(); ur1 = FMA(-pfg[1], ub, zr1); PIN(ur1); SBAR();
    SBAR();
    asm volatile("s_waitcnt vmcnt(8)\n\ts_barrier" ::: "memory"); SBAR();
    acc2 = MFMA(xf[12], wf0, acc2);
    SBAR();
    rb2 += 49152; PIN(rb2); wf2 = ldb128(rb2 + 0);
    SBAR();
    ui1 = FMA(-pfg[0], ub, ui1); PIN(ui1); SBAR(); ur1 = FMA(pfg[0], ua, ur1); PIN(ur1); SBAR(); oi0 = cvtpk(ui0, ui1); PIN(oi0); SBAR(); or0 = cvtpk(ur0, ur1); PIN(or0); SBAR();
    SBAR();
    t2_ = pfl[1] * zi1; PIN(t2_); SBAR(); t3_ = pfl[1] * zr1; PIN(t3_); SBAR(); ua = bflo(hlr[5]); PIN(ua); SBAR(); ub = bflo(hli[5]); PIN(ub); SBAR();
    SBAR();
    zi0 = FMA(pfl[0], zi1, t3_); PIN(zi0); SBAR(); zr0 = FMA(pfl[0], zr1, -t2_); PIN(zr0); SBAR();
    SBAR();
    acc2 = MFMA(xf[13], wf1, acc2);
    SBAR();
    rb3 += 49152; PIN(rb3); wf3 = ldb128(rb3 + 0);
    SBAR();
    ui0 = FMA(-pfg[1], ua, -zi0); PIN(ui0); SBAR(); ur0 = FMA(-pfg[1], ub, zr0); PIN(ur0); SBAR();
    SBAR();
    ui0 = FMA(-pfg[0], ub, ui0); PIN(ui0); SBAR(); ur0 = FMA(pfg[0], ua, ur0); PIN(ur0); SBAR();
    SBAR();
    GL(gcr, ROT9(3) * 73728 + 0, voffR, 73728);
    SBAR();
    acc2 = MFMA(xf[14], wf2, acc2);
    SBAR();
    wf4 = ldb128(rb0 + 8192);
    SBAR();
    t2_ = pfl[1] * zi0; PIN(t2_); SBAR(); t3_ = pfl[1] * zr0; PIN(t3_); SBAR(); ua = bfhi(hlr[5]); PIN(ua); SBAR(); ub = bfhi(hli[5]); PIN(ub); SBAR();
    SBAR();
    zi1 = FMA(pfl[0], zi0, t3_); PIN(zi1); SBAR(); zr1 = FMA(pfl[0], zr0, -t2_); PIN(zr1); SBAR();
    SBAR();
    ui1 = FMA(-pfg[1], ua, -zi1); PIN(ui1); SBAR(); ur1 = FMA(-pfg[1], ub, zr1); PIN(ur1); SBAR();
    SBAR();
    acc2 = MFMA(xf[15], wf3, acc2);
    SBAR();
    wf5 = ldb128(rb1 + 8192);
    SBAR();
    ui1 = FMA(-pfg[0], ub, ui1); PIN(ui1); SBAR(); ur1 = FMA(pfg[0], ua, ur1); PIN(ur1); SBAR(); oi1 = cvtpk(ui0, ui1); PIN(oi1); SBAR(); or1 = cvtpk(ur0, ur1); PIN(or1); SBAR();
    SBAR();
    t2_ = pfl[1] * zi1; PIN(t2_); SBAR(); t3_ = pfl[1] * zr1; PIN(t3_); SBAR(); ua = bflo(hlr[6]); PIN(ua); SBAR(); ub = bflo(hli[6]); PIN(ub); SBAR();
    SBAR();
    acc2 = MFMA(xf[16], wf4, acc2);
    SBAR();
    wf6 = ldb128(rb2 + 8192);
    SBAR();
    zi0 = FMA(pfl[0], zi1, t3_); PIN(zi0); SBAR(); zr0 = FMA(pfl[0], zr1, -t2_); PIN(zr0); SBAR();
    SBAR();
    ui0 = FMA(-pfg[1], ua, -zi0); PIN(ui0); SBAR(); ur0 = FMA(-pfg[1], ub, zr0); PIN(ur0); SBAR();
    SBAR();
    ui0 = FMA(-pfg[0], ub, ui0); PIN(ui0); SBAR(); ur0 = FMA(pfg[0], ua, ur0); PIN(ur0); SBAR();
    SBAR();
    acc2 = MFMA(xf[17], wf5, acc2);
    SBAR();
    wf7 = ldb128(rb3 + 8192);
    SBAR();
    t2_ = pfl[1] * zi0; PIN(t2_); SBAR(); t3_ = pfl[1] * zr0; PIN(t3_); SBAR(); ua = bfhi(hlr[6]); PIN(ua); SBAR(); ub = bfhi(hli[6]); PIN(ub); SBAR();
    SBAR();
    zi1 = FMA(pfl[0], zi0, t3_); PIN(zi1); SBAR(); zr1 = FMA(pfl[0], zr0, -t2_); PIN(zr1); SBAR();
    SBAR();
    GL(gci, ROT9(3) * 73728 + 0, voffR, 81920);
    SBAR();
    acc2 = MFMA(xf[18], wf6, acc2);
    SBAR();
    wf8 = ldb128(rb0 + 16384);
    SBAR();
    ui1 = FMA(-pfg[1], ua, -zi1); PIN(ui1); SBAR(); ur1 = FMA(-pfg[1], ub, zr1); PIN(ur1); SBAR();
    SBAR();
    ui1 = FMA(-pfg[0], ub, ui1); PIN(ui1); SBAR(); ur1 = FMA(pfg[0], ua, ur1); PIN(ur1); SBAR(); oi2 = cvtpk(ui0, ui1); PIN(oi2); SBAR(); or2 = cvtpk(ur0, ur1); PIN(or2); SBAR();
    SBAR();
    t2_ = pfl[1] * zi1; PIN(t2_); SBAR(); t3_ = pfl[1] * zr1; PIN(t3_); SBAR(); ua = bflo(hlr[7]); PIN(ua); SBAR(); ub = bflo(hli[7]); PIN(ub); SBAR();
    SBAR();
    acc2 = MFMA(xf[19], wf7, acc2);
    SBAR();
    wf9 = ldb128(rb1 + 16384);
    SBAR();
    zi0 = FMA(pfl[0], zi1, t3_); PIN(zi0); SBAR(); zr0 = FMA(pfl[0], zr1, -t2_); PIN(zr0); SBAR();
    SBAR();
    ui0 = FMA(-pfg[1], ua, -zi0); PIN(ui0); SBAR(); ur0 = FMA(-pfg[1], ub, zr0); PIN(ur0); SBAR();
    SBAR();
    acc2 = MFMA(xf[20], wf8, acc2);
    SBAR();
    wf10 = ldb128(rb2 + 16384);
    SBAR();
    ui0 = FMA(-pfg[0], ub, ui0); PIN(ui0); SBAR(); ur0 = FMA(pfg[0], ua, ur0); PIN(ur0); SBAR();
    SBAR();
    t2_ = pfl[1] * zi0; PIN(t2_); SBAR(); t3_ = pfl[1] * zr0; PIN(t3_); SBAR(); ua = bfhi(hlr[7]); PIN(ua); SBAR(); ub = bfhi(hli[7]); PIN(ub); SBAR();
    SBAR();
    zi1 = FMA(pfl[0], zi0, t3_); PIN(zi1); SBAR(); zr1 = FMA(pfl[0], zr0, -t2_); PIN(zr1); SBAR();
    SBAR();
    acc2 = MFMA(xf[21], wf9, acc2);
    SBAR();
    wf11 = ldb128(rb3 + 16384);
    SBAR();
    ui1 = FMA(-pfg[1], ua, -zi1); PIN(ui1); SBAR(); ur1 = FMA(-pfg[1], ub, zr1); PIN(ur1); SBAR();
    SBAR();
    ui1 = FMA(-pfg[0], ub, ui1); PIN(ui1); SBAR(); ur1 = FMA(pfg[0], ua, ur1); PIN(ur1); SBAR(); oi3 = cvtpk(ui0, ui1); PIN(oi3); SBAR(); or3 = cvtpk(ur0, ur1); PIN(or3); SBAR(); { const lptr p_ = HW(1); stu4(p_ + 4096, (u32x4){oi0, oi1, oi2, oi3}); stu4(p_, (u32x4){or0, or1, or2, or3}); }
    SBAR();
    GL(gcr, ROT9(4) * 73728 + 0, voffR, 90112);
    SBAR();
    acc2 = MFMA(xf[22], wf10, acc2);
    SBAR();
    rb0 += -98304; PIN(rb0); wf0 = ldb128(rb0 + 0);
    rb1 += -98304; PIN(rb1); wf1 = ldb128(rb1 + 0);
    SBAR();
    acc2 = MFMA(xf[23], wf11, acc2);
    SBAR();
    asm volatile("s_waitcnt vmcnt(8) lgkmcnt(2)\n\ts_barrier" ::: "memory"); SBAR();
    acc2 = MFMA(xf[24], wf0, acc2);
    SBAR();
    rb2 += -98304; PIN(rb2); wf2 = ldb128(rb2 + 0);
    SBAR();
    MK_HT();
    SBAR();
    LB_ADDR(); ALD4(g0, lbo0, ggr + 16384); ALD4(g1, lbo1, ggr + 16384); ALD4(tc, voffC, gpr + 7168);
    SBAR();
    acc2 = MFMA(xf[25], wf1, acc2);
    SBAR();
    rb3 += -98304; PIN(rb3); wf3 = ldb128(rb3 + 0);
    SBAR();
    hs[0] = trfrag2(ht0 + 0, ht1 + 0);
    SBAR();
    GL(gci, ROT9(4) * 73728 + 0, voffR, 98304);
    SBAR();
    acc2 = MFMA(xf[26], wf2, acc2);
    SBAR();
    wf4 = ldb128(rb0 + 8192);
    SBAR();
    hs[1] = trfrag2(ht0 + 1024, ht1 + 1024);
    SBAR();
    acc2 = MFMA(xf[27], wf3, acc2);
    SBAR();
    wf5 = ldb128(rb1 + 8192);
    SBAR();
    hs[2] = trfrag2(ht0 + 2048, ht1 + 2048);
    SBAR();
    acc2 = MFMA(xf[28], wf4, acc2);
    SBAR();
    wf6 = ldb128(rb2 + 8192);
    SBAR();
    hs[3] = trfrag2(ht0 + 3072, ht1 + 3072);
    SBAR();
    acc2 = MFMA(xf[29], wf5, acc2);
    SBAR();
    wf7 = ldb128(rb3 + 8192);
    SBAR();
    hs[4] = trfrag2(ht0 + 4096, ht1 + 4096);
    SBAR();
    GL(gcr, ROT9(5) * 73728 + 0, voffR, 106496);
    SBAR();
    acc2 = MFMA(xf[30], wf6, acc2);
    SBAR();
    wf8 = ldb128(rb0 + 16384);
    SBAR();
    VWAIT1(9, cor2); CHK_COR(cor2, 4);
    SBAR();
    hs[5] = trfrag2(ht0 + 5120, ht1 + 5120);
    SBAR();
    acc2 = MFMA(xf[31], wf7, acc2);
    SBAR();
    wf9 = ldb128(rb1 + 16384);
    SBAR();
    VWAIT1(8, cor); CHK_COR(cor, 5);
    SBAR();
    hs[6] = trfrag2(ht0 + 6144, ht1 + 6144);
    SBAR();
    acc2 = MFMA(xf[32], wf8, acc2);
    SBAR();
    wf10 = ldb128(rb2 + 16384);
    SBAR();
    hs[7] = trfrag2(ht0 + 7168, ht1 + 7168);
    SBAR();
    prm = ldf2(sq + 38400); MK_PT();
    SBAR();
    acc1 = MFMA(lnf, CORF2, acc1);
    SBAR();
    acc2 = MFMA(xf[33], wf9, acc2);
    SBAR();
    wf11 = ldb128(rb3 + 16384);
    SBAR();
    GL(gci, ROT9(5) * 73728 + 0, voffR, 114688);
    SBAR();
    acc2 = MFMA(lnf, CORF, acc2);
    SBAR();
    acc2 = MFMA(xf[34], wf10, acc2);
    SBAR();
    wf0 = ldb128(rb0 + 24576);
    wf1 = ldb128(rb1 + 24576);
    SBAR();
    acc2 = MFMA(xf[35], wf11, acc2);
    SBAR();
    rt40 = ldf2(pt + 0);
    SBAR();
    asm volatile("s_waitcnt vmcnt(11)\n\ts_barrier" ::: "memory"); SBAR();
    bcc[0] = MFMA(wf0, hs[0], zero16);
    SBAR();
    wf2 = ldb128(rb2 + 24576);
    SBAR();
    bcc[0] = MFMA(wf1, hs[1], bcc[0]);
    SBAR();
    wf3 = ldb128(rb3 + 24576);
    SBAR();
    VWAIT3(3, g0, g1, tc); LB_CHECK(1);
    SBAR();
    hr0 = acc1[0] * rt40[0]; PIN(hr0); SBAR(); hi0 = acc2[0] * rt40[0]; PIN(hi0); SBAR();
    SBAR();
    rt41 = ldf2(pt + 8);
    SBAR();
    GL(gcr, ROT9(6) * 73728 + 0, voffR, 0);
    SBAR();
    bcc[0] = MFMA(wf2, hs[2], bcc[0]);
    SBAR();
    wf4 = ldb128(rb0 + 32768);
    SBAR();
    t0_ = FMA(-TBF(tc, 1), TBF(g1, 2), TBF(g0, 0)); PIN(t0_); SBAR(); t1_ = FMA(TBF(tc, 1), TBF(g1, 0), TBF(g0, 2)); PIN(t1_); SBAR(); t0_ = FMA(TBF(tc, 0), TBF(g1, 0), t0_); PIN(t0_); SBAR(); t1_ = FMA(TBF(tc, 0), TBF(g1, 2), t1_); PIN(t1_); SBAR();
    SBAR();
    vr1 = acc1[1] * rt40[1]; PIN(vr1); SBAR(); vi1 = acc2[1] * rt40[1]; PIN(vi1); SBAR();
    SBAR();
    bcc[0] = MFMA(wf3, hs[3], bcc[0]);
    SBAR();
    wf5 = ldb128(rb1 + 32768);
    SBAR();
    stf2(pw, t0_, t1_);
    SBAR();
    vi1 = FMA(prm[1], hr0, vi1); PIN(vi1); SBAR(); vr1 = FMA(-prm[1], hi0, vr1); PIN(vr1); SBAR();
    SBAR();
    vr0 = acc1[2] * rt41[0]; PIN(vr0); SBAR(); vi0 = acc2[2] * rt41[0]; PIN(vi0); SBAR();
    SBAR();
    rt40 = ldf2(pt + 16);
    SBAR();
    bcc[0] = MFMA(wf4, hs[4], bcc[0]);
    SBAR();
    wf6 = ldb128(rb2 + 32768);
    SBAR();
    hi1 = FMA(prm[0], hi0, vi1); PIN(hi1); SBAR(); hr1 = FMA(prm[0], hr0, vr1); PIN(hr1); SBAR();
    SBAR();
    hli[0] = cvtpk(hi0, hi1); PIN(hli[0]); SBAR(); hlr[0] = cvtpk(hr0, hr1); PIN(hlr[0]); SBAR();
    SBAR();
    vi0 = FMA(prm[1], hr1, vi0); PIN(vi0); SBAR(); vr0 = FMA(-prm[1], hi1, vr0); PIN(vr0); SBAR();
    SBAR();
    vr1 = acc1[3] * rt41[1]; PIN(vr1); SBAR(); vi1 = acc2[3] * rt41[1]; PIN(vi1); SBAR();
    SBAR();
    bcc[0] = MFMA(wf5, hs[5], bcc[0]);
    SBAR();
    wf7 = ldb128(rb3 + 32768);
    SBAR();
    hr0 = FMA(prm[0], hr1, vr0); PIN(hr0); SBAR(); hi0 = FMA(prm[0], hi1, vi0); PIN(hi0); SBAR();
    SBAR();
    vi1 = FMA(prm[1], hr0, vi1); PIN(vi1); SBAR(); vr1 = FMA(-prm[1], hi0, vr1); PIN(vr1); SBAR();
    SBAR();
    vr0 = acc1[4] * rt40[0]; PIN(vr0); SBAR(); vi0 = acc2[4] * rt40[0]; PIN(vi0); SBAR();
    SBAR();
    GL(gci, ROT9(6) * 73728 + 0, voffR, 8192);
    SBAR();
    bcc[0] = MFMA(wf6, hs[6], bcc[0]);
    SBAR();
    wf8 = ldb128(rb0 + 40960);
    SBAR();
    hi1 = FMA(prm[0], hi0, vi1); PIN(hi1); SBAR(); hr1 = FMA(prm[0], hr0, vr1); PIN(hr1); SBAR();
    SBAR();
    hli[1] = cvtpk(hi0, hi1); PIN(hli[1]); SBAR(); hlr[1] = cvtpk(hr0, hr1); PIN(hlr[1]); SBAR();
    SBAR();
    vi0 = FMA(prm[1], hr1, vi0); PIN(vi0); SBAR(); vr0 = FMA(-prm[1], hi1, vr0); PIN(vr0); SBAR();
    SBAR();
    rt41 = ldf2(pt + 24);
    SBAR();
    bcc[0] = MFMA(wf7, hs[7], bcc[0]);
    SBAR();
    wf9 = ldb128(rb1 + 40960);
    SBAR();
    hr0 = FMA(prm[0], hr1, vr0); PIN(hr0); SBAR(); hi0 = FMA(prm[0], hi1, vi0); PIN(hi0); SBAR();
    SBAR();
    vr1 = acc1[5] * rt40[1]; PIN(vr1); SBAR(); vi1 = acc2[5] * rt40[1]; PIN(vi1); SBAR();
    SBAR();
    bcc[1] = MFMA(wf8, hs[0], zero16);
    SBAR();
    wf10 = ldb128(rb2 + 40960);
    SBAR();
    MK_S1(); ALD4(tb0, voffS1, gpr + 11264);
    SBAR();
    vi1 = FMA(prm[1], hr0, vi1); PIN(vi1); SBAR(); vr1 = FMA(-prm[1], hi0, vr1); PIN(vr1); SBAR();
    SBAR();
    vr0 = acc1[6] * rt41[0]; PIN(vr0); SBAR(); vi0 = acc2[6] * rt41[0]; PIN(vi0); SBAR();
    SBAR();
    rt40 = ldf2(pt + 32);
    SBAR();
    bcc[1] = MFMA(wf9, hs[1], bcc[1]);
    SBAR();
    wf11 = ldb128(rb3 + 40960);
    SBAR();
    hi1 = FMA(prm[0], hi0, vi1); PIN(hi1); SBAR(); hr1 = FMA(prm[0], hr0, vr1); PIN(hr1); SBAR();
    SBAR();
    hli[2] = cvtpk(hi0, hi1); PIN(hli[2]); SBAR(); hlr[2] = cvtpk(hr0, hr1); PIN(hlr[2]); SBAR();
    SBAR();
    vi0 = FMA(prm[1], hr1, vi0); PIN(vi0); SBAR(); vr0 = FMA(-prm[1], hi1, vr0); PIN(vr0); SBAR();
    SBAR();
    vr1 = acc1[7] * rt41[1]; PIN(vr1); SBAR(); vi1 = acc2[7] * rt41[1]; PIN(vi1); SBAR();
    SBAR();
    GL(gcr, ROT9(7) * 73728 + 0, voffR, 16384);
    SBAR();
    bcc[1] = MFMA(wf10, hs[2], bcc[1]);
    SBAR();
    rb0 += 49152; PIN(rb0); wf0 = ldb128(rb0 + 0);
    rb1 += 49152; PIN(rb1); wf1 = ldb128(rb1 + 0);
    SBAR();
    hr0 = FMA(prm[0], hr1, vr0); PIN(hr0); SBAR(); hi0 = FMA(prm[0], hi1, vi0); PIN(hi0); SBAR();
    SBAR();
    vi1 = FMA(prm[1], hr0, vi1); PIN(vi1); SBAR(); vr1 = FMA(-prm[1], hi0, vr1); PIN(vr1); SBAR();
    SBAR();
    vr0 = acc1[8] * rt40[0]; PIN(vr0); SBAR(); vi0 = acc2[8] * rt40[0]; PIN(vi0); SBAR();
    SBAR();
    bcc[1] = MFMA(wf11, hs[3], bcc[1]);
    SBAR();
    hi1 = FMA(prm[0], hi0, vi1); PIN(hi1); SBAR(); hr1 = FMA(prm[0], hr0, vr1); PIN(hr1); SBAR();
    SBAR();
    hli[3] = cvtpk(hi0, hi1); PIN(hli[3]); SBAR(); hlr[3] = cvtpk(hr0, hr1); PIN(hlr[3]); SBAR();
    SBAR();
    vi0 = FMA(prm[1], hr1, vi0); PIN(vi0); SBAR(); vr0 = FMA(-prm[1], hi1, vr0); PIN(vr0); SBAR();
    SBAR();
    vr1 = acc1[9] * rt40[1]; PIN(vr1); SBAR(); vi1 = acc2[9] * rt40[1]; PIN(vi1); SBAR();
    SBAR();
    rt41 = ldf2(pt + 40);
    SBAR();
    asm volatile("s_waitcnt vmcnt(10) lgkmcnt(2)\n\ts_barrier" ::: "memory"); SBAR();
    bcc[1] = MFMA(wf0, hs[4], bcc[1]);
    SBAR();
    rb2 += 49152; PIN(rb2); wf2 = ldb128(rb2 + 0);
    SBAR();
    fa0 = ldf2(sq + 3584); fb0 = ldf2(sq + 3072);
    SBAR();
    hr0 = FMA(prm[0], hr1, vr0); PIN(hr0); SBAR(); hi0 = FMA(prm[0], hi1, vi0); PIN(hi0); SBAR();
    SBAR();
    vi1 = FMA(prm[1], hr0, vi1); PIN(vi1); SBAR(); vr1 = FMA(-prm[1], hi0, vr1); PIN(vr1); SBAR();
    SBAR();
    bcc[1] = MFMA(wf1, hs[5], bcc[1]);
    SBAR();
    rb3 += 49152; PIN(rb3); wf3 = ldb128(rb3 + 0);
    SBAR();
    fa1 = ldf2(sq + 2560); fb1 = ldf2(sq + 2048);
    SBAR();
    hi1 = FMA(prm[0], hi0, vi1); PIN(hi1); SBAR(); hr1 = FMA(prm[0], hr0, vr1); PIN(hr1); SBAR();
    SBAR();
    hli[4] = cvtpk(hi0, hi1); PIN(hli[4]); SBAR(); hlr[4] = cvtpk(hr0, hr1); PIN(hlr[4]); SBAR();
    SBAR();
    vr0 = acc1[10] * rt41[0]; PIN(vr0); SBAR(); vi0 = acc2[10] * rt41[0]; PIN(vi0); SBAR();
    SBAR();
    rt40 = ldf2(pt + 48);
    SBAR();
    GL(gci, ROT9(7) * 73728 + 0, voffR, 24576);
    SBAR();
    bcc[1] = MFMA(wf2, hs[6], bcc[1]);
    SBAR();
    wf4 = ldb128(rb0 + 8192);
    SBAR();
    Cr = fa0.x; Ci = fa0.y;
    SBAR();
    vi0 = FMA(prm[1], hr1, vi0); PIN(vi0); SBAR(); vr0 = FMA(-prm[1], hi1, vr0); PIN(vr0); SBAR();
    SBAR();
    vr1 = acc1[11] * rt41[1]; PIN(vr1); SBAR(); vi1 = acc2[11] * rt41[1]; PIN(vi1); SBAR();
    SBAR();
    bcc[1] = MFMA(wf3, hs[7], bcc[1]);
    SBAR();
    wf5 = ldb128(rb1 + 8192);
    SBAR();
    t2_ = FMA(-TBF(tc, 3), Ci, fb0.x); PIN(t2_); SBAR(); t3_ = FMA(TBF(tc, 3), Cr, fb0.y); PIN(t3_); SBAR(); Cr = FMA(TBF(tc, 2), Cr, t2_); PIN(Cr); SBAR(); Ci = FMA(TBF(tc, 2), Ci, t3_); PIN(Ci); SBAR();
    SBAR();
    hr0 = FMA(prm[0], hr1, vr0); PIN(hr0); SBAR(); hi0 = FMA(prm[0], hi1, vi0); PIN(hi0); SBAR();
    SBAR();
    vi1 = FMA(prm[1], hr0, vi1); PIN(vi1); SBAR(); vr1 = FMA(-prm[1], hi0, vr1); PIN(vr1); SBAR();
    SBAR();
    vr0 = acc1[12] * rt40[0]; PIN(vr0); SBAR(); vi0 = acc2[12] * rt40[0]; PIN(vi0); SBAR();
    SBAR();
    rt41 = ldf2(pt + 56);
    SBAR();
    bcc[2] = MFMA(wf4, hs[0], zero16);
    SBAR();
    wf6 = ldb128(rb2 + 8192);
    SBAR();
    t2_ = FMA(-TBF(tc, 3), Ci, fa1.x); PIN(t2_); SBAR(); t3_ = FMA(TBF(tc, 3), Cr, fa1.y); PIN(t3_); SBAR(); Cr = FMA(TBF(tc, 2), Cr, t2_); PIN(Cr); SBAR(); Ci = FMA(TBF(tc, 2), Ci, t3_); PIN(Ci); SBAR();
    SBAR();
    fa0 = ldf2(sq + 1536); fb0 = ldf2(sq + 1024);
    SBAR();
    hi1 = FMA(prm[0], hi0, vi1); PIN(hi1); SBAR(); hr1 = FMA(prm[0], hr0, vr1); PIN(hr1); SBAR();
    SBAR();
    hli[5] = cvtpk(hi0, hi1); PIN(hli[5]); SBAR(); hlr[5] = cvtpk(hr0, hr1); PIN(hlr[5]); SBAR();
    SBAR();
    vi0 = FMA(prm[1], hr1, vi0); PIN(vi0); SBAR(); vr0 = FMA(-prm[1], hi1, vr0); PIN(vr0); SBAR();
    SBAR();
    vr1 = acc1[13] * rt40[1]; PIN(vr1); SBAR(); vi1 = acc2[13] * rt40[1]; PIN(vi1); SBAR();
    SBAR();
    bcc[2] = MFMA(wf5, hs[1], bcc[2]);
    SBAR();
    wf7 = ldb128(rb3 + 8192);
    SBAR();
    t2_ = FMA(-TBF(tc, 3), Ci, fb1.x); PIN(t2_); SBAR(); t3_ = FMA(TBF(tc, 3), Cr, fb1.y); PIN(t3_); SBAR(); Cr = FMA(TBF(tc, 2), Cr, t2_); PIN(Cr); SBAR(); Ci = FMA(TBF(tc, 2), Ci, t3_); PIN(Ci); SBAR();
    SBAR();
    hr0 = FMA(prm[0], hr1, vr0); PIN(hr0); SBAR(); hi0 = FMA(prm[0], hi1, vi0); PIN(hi0); SBAR();
    SBAR();
    vi1 = FMA(prm[1], hr0, vi1); PIN(vi1); SBAR(); vr1 = FMA(-prm[1], hi0, vr1); PIN(vr1); SBAR();
    SBAR();
    vr0 = acc1[14] * rt41[0]; PIN(vr0); SBAR(); vi0 = acc2[14] * rt41[0]; PIN(vi0); SBAR();
    SBAR();
    GL(gcr, ROT9(8) * 73728 + 0, voffR, 32768);
    SBAR();
    bcc[2] = MFMA(wf6, hs[2], bcc[2]);
    SBAR();
    wf8 = ldb128(rb0 + 16384);
    SBAR();
    t2_ = FMA(-TBF(tc, 3), Ci, fa0.x); PIN(t2_); SBAR(); t3_ = FMA(TBF(tc, 3), Cr, fa0.y); PIN(t3_); SBAR(); Cr = FMA(TBF(tc, 2), Cr, t2_); PIN(Cr); SBAR(); Ci = FMA(TBF(tc, 2), Ci, t3_); PIN(Ci); SBAR();
    SBAR();
    fa1 = ldf2(sq + 512); fb1 = ldf2(sq + 0);
    SBAR();
    hi1 = FMA(prm[0], hi0, vi1); PIN(hi1); SBAR(); hr1 = FMA(prm[0], hr0, vr1); PIN(hr1); SBAR();
    SBAR();
    hli[6] = cvtpk(hi0, hi1); PIN(hli[6]); SBAR(); hlr[6] = cvtpk(hr0, hr1); PIN(hlr[6]); SBAR();
    SBAR();
    vi0 = FMA(prm[1], hr1, vi0); PIN(vi0); SBAR(); vr0 = FMA(-prm[1], hi1, vr0); PIN(vr0); SBAR();
    SBAR();
    bcc[2] = MFMA(wf7, hs[3], bcc[2]);
    SBAR();
    wf9 = ldb128(rb1 + 16384);
    SBAR();
    t2_ = FMA(-TBF(tc, 3), Ci, fb0.x); PIN(t2_); SBAR(); t3_ = FMA(TBF(tc, 3), Cr, fb0.y); PIN(t3_); SBAR(); Cr = FMA(TBF(tc, 2), Cr, t2_); PIN(Cr); SBAR(); Ci = FMA(TBF(tc, 2), Ci, t3_); PIN(Ci); SBAR();
    SBAR();
    hr0 = FMA(prm[0], hr1, vr0); PIN(hr0); SBAR(); hi0 = FMA(prm[0], hi1, vi0); PIN(hi0); SBAR();
    SBAR();
    vr1 = acc1[15] * rt41[1]; PIN(vr1); SBAR(); vi1 = acc2[15] * rt41[1]; PIN(vi1); SBAR();
    SBAR();
    bcc[2] = MFMA(wf8, hs[4], bcc[2]);
    SBAR();
    wf10 = ldb128(rb2 + 16384);
    SBAR();
    t2_ = FMA(-TBF(tc, 3), Ci, fa1.x); PIN(t2_); SBAR(); t3_ = FMA(TBF(tc, 3), Cr, fa1.y); PIN(t3_); SBAR(); Cr = FMA(TBF(tc, 2), Cr, t2_); PIN(Cr); SBAR(); Ci = FMA(TBF(tc, 2), Ci, t3_); PIN(Ci); SBAR();
    SBAR();
    pfl = ldf2(sq + 37888); pfg = ldf2(sq + 39424);
    SBAR();
    vi1 = FMA(prm[1], hr0, vi1); PIN(vi1); SBAR(); vr1 = FMA(-prm[1], hi0, vr1); PIN(vr1); SBAR();
    SBAR();
    bcc[2] = MFMA(wf9, hs[5], bcc[2]);
    SBAR();
    wf11 = ldb128(rb3 + 16384);
    SBAR();
    t2_ = FMA(-TBF(tc, 3), Ci, fb1.x); PIN(t2_); SBAR(); t3_ = FMA(TBF(tc, 3), Cr, fb1.y); PIN(t3_); SBAR(); Cr = FMA(TBF(tc, 2), Cr, t2_); PIN(Cr); SBAR(); Ci = FMA(TBF(tc, 2), Ci, t3_); PIN(Ci); SBAR();
    SBAR();
    hi1 = FMA(prm[0], hi0, vi1); PIN(hi1); SBAR(); hr1 = FMA(prm[0], hr0, vr1); PIN(hr1); SBAR();
    SBAR();
    hli[7] = cvtpk(hi0, hi1); PIN(hli[7]); SBAR(); hlr[7] = cvtpk(hr0, hr1); PIN(hlr[7]); SBAR();
    SBAR();
    stf2(pw + 32768, hr1, hi1);
    SBAR();
    GL(gci, ROT9(8) * 73728 + 0, voffR, 40960);
    SBAR();
    bcc[2] = MFMA(wf10, hs[6], bcc[2]);
    SBAR();
    wf0 = ldb128(rb0 + 24576);
    wf1 = ldb128(rb1 + 24576);
    SBAR();
    t2_ = FMA(-TBF(tb1, 3), Ci, Pwr); PIN(t2_); SBAR(); t3_ = FMA(TBF(tb1, 3), Cr, Pwi); PIN(t3_); SBAR(); Cr = FMA(TBF(tb1, 2), Cr, t2_); PIN(Cr); SBAR(); Ci = FMA(TBF(tb1, 2), Ci, t3_); PIN(Ci); SBAR();
    SBAR();
    bcc[2] = MFMA(wf11, hs[7], bcc[2]);
    SBAR();
    asm volatile("s_waitcnt vmcnt(7) lgkmcnt(2)\n\ts_barrier" ::: "memory"); SBAR();
    bcc[3] = MFMA(wf0, hs[0], zero16);
    SBAR();
    wf2 = ldb128(rb2 + 24576);
    SBAR();
    t2_ = pfg[1] * Ci; PIN(t2_); SBAR(); t3_ = pfg[1] * Cr; PIN(t3_); SBAR(); zr1 = FMA(pfg[0], Cr, -t2_); PIN(zr1); SBAR(); zi1 = FMA(pfg[0], Ci, t3_); PIN(zi1); SBAR();
    SBAR();
    ea0 = ldf2(sq + 32768); eb0 = ldf2(sq + 33280);
    SBAR();
    bcc[3] = MFMA(wf1, hs[1], bcc[3]);
    SBAR();
    wf3 = ldb128(rb3 + 24576);
    SBAR();
    ea1 = ldf2(sq + 33792); eb1 = ldf2(sq + 34304);
    SBAR();
    GL(gcr, ROT9(0) * 73728 + 128, voffR, 49152);
    SBAR();
    bcc[3] = MFMA(wf2, hs[2], bcc[3]);
    SBAR();
    wf4 = ldb128(rb0 + 32768);
    SBAR();
    t2_ = pfl[1] * zi1; PIN(t2_); SBAR(); t3_ = pfl[1] * zr1; PIN(t3_); SBAR(); ua = bflo(hlr[8]); PIN(ua); SBAR(); ub = bflo(hli[8]); PIN(ub); SBAR();
    SBAR();
    VWAIT1(5, tb0);
    SBAR();
    Pr = ea0.x; Pi = ea0.y; Pwr = seg > 0 ? Pr : 0.f; PIN(Pwr); SBAR(); Pwi = seg > 0 ? Pi : 0.f; PIN(Pwi); SBAR();
    SBAR();
    bcc[3] = MFMA(wf3, hs[3], bcc[3]);
    SBAR();
    wf5 = ldb128(rb1 + 32768);
    SBAR();
    zi0 = FMA(pfl[0], zi1, t3_); PIN(zi0); SBAR(); zr0 = FMA(pfl[0], zr1, -t2_); PIN(zr0); SBAR();
    SBAR();
    t0_ = FMA(-TBF(tb0, 1), Pi, eb0.x); PIN(t0_); SBAR(); t1_ = FMA(TBF(tb0, 1), Pr, eb0.y); PIN(t1_); SBAR(); Pr = FMA(TBF(tb0, 0), Pr, t0_); PIN(Pr); SBAR(); Pi = FMA(TBF(tb0, 0), Pi, t1_); PIN(Pi); SBAR(); Pwr = seg > 1 ? Pr : Pwr; PIN(Pwr); SBAR(); Pwi = seg > 1 ? Pi : Pwi; PIN(Pwi); SBAR();
    SBAR();
    bcc[3] = MFMA(wf4, hs[4], bcc[3]);
    SBAR();
    wf6 = ldb128(rb2 + 32768);
    SBAR();
    ui0 = FMA(-pfg[1], ua, -zi0); PIN(ui0); SBAR(); ur0 = FMA(-pfg[1], ub, zr0); PIN(ur0); SBAR();
    SBAR();
    ea0 = ldf2(sq + 34816); eb0 = ldf2(sq + 35328);
    SBAR();
    t0_ = FMA(-TBF(tb0, 1), Pi, ea1.x); PIN(t0_); SBAR(); t1_ = FMA(TBF(tb0, 1), Pr, ea1.y); PIN(t1_); SBAR(); Pr = FMA(TBF(tb0, 0), Pr, t0_); PIN(Pr); SBAR(); Pi = FMA(TBF(tb0, 0), Pi, t1_); PIN(Pi); SBAR(); Pwr = seg > 2 ? Pr : Pwr; PIN(Pwr); SBAR(); Pwi = seg > 2 ? Pi : Pwi; PIN(Pwi); SBAR();
    SBAR();
    bcc[3] = MFMA(wf5, hs[5], bcc[3]);
    SBAR();
    wf7 = ldb128(rb3 + 32768);
    SBAR();
    ui0 = FMA(-pfg[0], ub, ui0); PIN(ui0); SBAR(); ur0 = FMA(pfg[0], ua, ur0); PIN(ur0); SBAR();
    SBAR();
    t0_ = FMA(-TBF(tb0, 1), Pi, eb1.x); PIN(t0_); SBAR(); t1_ = FMA(TBF(tb0, 1), Pr, eb1.y); PIN(t1_); SBAR(); Pr = FMA(TBF(tb0, 0), Pr, t0_); PIN(Pr); SBAR(); Pi = FMA(TBF(tb0, 0), Pi, t1_); PIN(Pi); SBAR(); Pwr = seg > 3 ? Pr : Pwr; PIN(Pwr); SBAR(); Pwi = seg > 3 ? Pi : Pwi; PIN(Pwi); SBAR();
    SBAR();
    GL(gci, ROT9(0) * 73728 + 128, voffR, 57344);
    SBAR();
    bcc[3] = MFMA(wf6, hs[6], bcc[3]);
    SBAR();
    wf8 = ldb128(rb0 + 40960);
    SBAR();
    ea1 = ldf2(sq + 35840); eb1 = ldf2(sq + 36352);
    SBAR();
    t0_ = FMA(-TBF(tb0, 1), Pi, ea0.x); PIN(t0_); SBAR(); t1_ = FMA(TBF(tb0, 1), Pr, ea0.y); PIN(t1_); SBAR(); Pr = FMA(TBF(tb0, 0), Pr, t0_); PIN(Pr); SBAR(); Pi = FMA(TBF(tb0, 0), Pi, t1_); PIN(Pi); SBAR(); Pwr = seg > 4 ? Pr : Pwr; PIN(Pwr); SBAR(); Pwi = seg > 4 ? Pi : Pwi; PIN(Pwi); SBAR();
    SBAR();
    bcc[3] = MFMA(wf7, hs[7], bcc[3]);
    SBAR();
    wf9 = ldb128(rb1 + 40960);
    SBAR();
    t2_ = pfl[1] * zi0; PIN(t2_); SBAR(); t3_ = pfl[1] * zr0; PIN(t3_); SBAR(); ua = bfhi(hlr[8]); PIN(ua); SBAR(); ub = bfhi(hli[8]); PIN(ub); SBAR();
    SBAR();
    t0_ = FMA(-TBF(tb0, 1), Pi, eb0.x); PIN(t0_); SBAR(); t1_ = FMA(TBF(tb0, 1), Pr, eb0.y); PIN(t1_); SBAR(); Pr = FMA(TBF(tb0, 0), Pr, t0_); PIN(Pr); SBAR(); Pi = FMA(TBF(tb0, 0), Pi, t1_); PIN(Pi); SBAR(); Pwr = seg > 5 ? Pr : Pwr; PIN(Pwr); SBAR(); Pwi = seg > 5 ? Pi : Pwi; PIN(Pwi); SBAR();
    SBAR();
    bcc[4] = MFMA(wf8, hs[0], zero16);
    SBAR();
    wf10 = ldb128(rb2 + 40960);
    SBAR();
    zi1 = FMA(pfl[0], zi0, t3_); PIN(zi1); SBAR(); zr1 = FMA(pfl[0], zr0, -t2_); PIN(zr1); SBAR();
    SBAR();
    t0_ = FMA(-TBF(tb0, 1), Pi, ea1.x); PIN(t0_); SBAR(); t1_ = FMA(TBF(tb0, 1), Pr, ea1.y); PIN(t1_); SBAR(); Pr = FMA(TBF(tb0, 0), Pr, t0_); PIN(Pr); SBAR(); Pi = FMA(TBF(tb0, 0), Pi, t1_); PIN(Pi); SBAR(); Pwr = seg > 6 ? Pr : Pwr; PIN(Pwr); SBAR(); Pwi = seg > 6 ? Pi : Pwi; PIN(Pwi); SBAR();
    SBAR();
    bcc[4] = MFMA(wf9, hs[1], bcc[4]);
    SBAR();
    wf11 = ldb128(rb3 + 40960);
    SBAR();
    ui1 = FMA(-pfg[1], ua, -zi1); PIN(ui1); SBAR(); ur1 = FMA(-pfg[1], ub, zr1); PIN(ur1); SBAR();
    SBAR();
    t0_ = FMA(-TBF(tb0, 1), Pi, eb1.x); PIN(t0_); SBAR(); t1_ = FMA(TBF(tb0, 1), Pr, eb1.y); PIN(t1_); SBAR(); Pr = FMA(TBF(tb0, 0), Pr, t0_); PIN(Pr); SBAR(); Pi = FMA(TBF(tb0, 0), Pi, t1_); PIN(Pi); SBAR();
    SBAR();
    GL(gcr, ROT9(1) * 73728 + 128, voffR, 65536);
    SBAR();
    bcc[4] = MFMA(wf10, hs[2], bcc[4]);
    SBAR();
    rb0 += 49152; PIN(rb0); wf0 = ldb128(rb0 + 0);
    rb1 += 49152; PIN(rb1); wf1 = ldb128(rb1 + 0);
    SBAR();
    ui1 = FMA(-pfg[0], ub, ui1); PIN(ui1); SBAR(); ur1 = FMA(pfg[0], ua, ur1); PIN(ur1); SBAR(); oi0 = cvtpk(ui0, ui1); PIN(oi0); SBAR(); or0 = cvtpk(ur0, ur1); PIN(or0); SBAR();
    SBAR();
    PUBLISH(2);
    SBAR();
    bcc[4] = MFMA(wf11, hs[3], bcc[4]);
    SBAR();
    asm volatile("s_waitcnt vmcnt(6)\n\ts_barrier" ::: "memory"); SBAR();
    bcc[4] = MFMA(wf0, hs[4], bcc[4]);
    SBAR();
    rb2 += 49152; PIN(rb2); wf2 = ldb128(rb2 + 0);
    SBAR();
    t2_ = pfl[1] * zi1; PIN(t2_); SBAR(); t3_ = pfl[1] * zr1; PIN(t3_); SBAR(); ua = bflo(hlr[9]); PIN(ua); SBAR(); ub = bflo(hli[9]); PIN(ub); SBAR();
    SBAR();
    zi0 = FMA(pfl[0], zi1, t3_); PIN(zi0); SBAR(); zr0 = FMA(pfl[0], zr1, -t2_); PIN(zr0); SBAR();
    SBAR();
    ui0 = FMA(-pfg[1], ua, -zi0); PIN(ui0); SBAR(); ur0 = FMA(-pfg[1], ub, zr0); PIN(ur0); SBAR();
    SBAR();
    bcc[4] = MFMA(wf1, hs[5], bcc[4]);
    SBAR();
    rb3 += 49152; PIN(rb3); wf3 = ldb128(rb3 + 0);
    SBAR();
    ui0 = FMA(-pfg[0], ub, ui0); PIN(ui0); SBAR(); ur0 = FMA(pfg[0], ua, ur0); PIN(ur0); SBAR();
    SBAR();
    t2_ = pfl[1] * zi0; PIN(t2_); SBAR(); t3_ = pfl[1] * zr0; PIN(t3_); SBAR(); ua = bfhi(hlr[9]); PIN(ua); SBAR(); ub = bfhi(hli[9]); PIN(ub); SBAR();
    SBAR();
    zi1 = FMA(pfl[0], zi0, t3_); PIN(zi1); SBAR(); zr1 = FMA(pfl[0], zr0, -t2_); PIN(zr1); SBAR();
    SBAR();
    GL(gci, ROT9(1) * 73728 + 128, voffR, 73728);
    SBAR();
    bcc[4] = MFMA(wf2, hs[6], bcc[4]);
    SBAR();
    wf4 = ldb128(rb0 + 8192);
    SBAR();
    ui1 = FMA(-pfg[1], ua, -zi1); PIN(ui1); SBAR(); ur1 = FMA(-pfg[1], ub, zr1); PIN(ur1); SBAR();
    SBAR();
    ui1 = FMA(-pfg[0], ub, ui1); PIN(ui1); SBAR(); ur1 = FMA(pfg[0], ua, ur1); PIN(ur1); SBAR(); oi1 = cvtpk(ui0, ui1); PIN(oi1); SBAR(); or1 = cvtpk(ur0, ur1); PIN(or1); SBAR();
    SBAR();
    bcc[4] = MFMA(wf3, hs[7], bcc[4]);
    SBAR();
    wf5 = ldb128(rb1 + 8192);
    SBAR();
    t2_ = pfl[1] * zi1; PIN(t2_); SBAR(); t3_ = pfl[1] * zr1; PIN(t3_); SBAR(); ua = bflo(hlr[10]); PIN(ua); SBAR(); ub = bflo(hli[10]); PIN(ub); SBAR();
    SBAR();
    zi0 = FMA(pfl[0], zi1, t3_); PIN(zi0); SBAR(); zr0 = FMA(pfl[0], zr1, -t2_); PIN(zr0); SBAR();
    SBAR();
    ui0 = FMA(-pfg[1], ua, -zi0); PIN(ui0); SBAR(); ur0 = FMA(-pfg[1], ub, zr0); PIN(ur0); SBAR();
    SBAR();
    bcc[5] = MFMA(wf4, hs[0], zero16);
    SBAR();
    wf6 = ldb128(rb2 + 8192);
    SBAR();
    ui0 = FMA(-pfg[0], ub, ui0); PIN(ui0); SBAR(); ur0 = FMA(pfg[0], ua, ur0); PIN(ur0); SBAR();
    SBAR();
    t2_ = pfl[1] * zi0; PIN(t2_); SBAR(); t3_ = pfl[1] * zr0; PIN(t3_); SBAR(); ua = bfhi(hlr[10]); PIN(ua); SBAR(); ub = bfhi(hli[10]); PIN(ub); SBAR();
    SBAR();
    bcc[5] = MFMA(wf5, hs[1], bcc[5]);
    SBAR();
    wf7 = ldb128(rb3 + 8192);
    SBAR();
    zi1 = FMA(pfl[0], zi0, t3_); PIN(zi1); SBAR(); zr1 = FMA(pfl[0], zr0, -t2_); PIN(zr1); SBAR();
    SBAR();
    ui1 = FMA(-pfg[1], ua, -zi1); PIN(ui1); SBAR(); ur1 = FMA(-pfg[1], ub, zr1); PIN(ur1); SBAR();
    SBAR();
    ui1 = FMA(-pfg[0], ub, ui1); PIN(ui1); SBAR(); ur1 = FMA(pfg[0], ua, ur1); PIN(ur1); SBAR(); oi2 = cvtpk(ui0, ui1); PIN(oi2); SBAR(); or2 = cvtpk(ur0, ur1); PIN(or2); SBAR();
    SBAR();
    GL(gcr, ROT9(2) * 73728 + 128, voffR, 81920);
    SBAR();
    bcc[5] = MFMA(wf6, hs[2], bcc[5]);
    SBAR();
    wf8 = ldb128(rb0 + 16384);
    SBAR();
    t2_ = pfl[1] * zi1; PIN(t2_); SBAR(); t3_ = pfl[1] * zr1; PIN(t3_); SBAR(); ua = bflo(hlr[11]); PIN(ua); SBAR(); ub = bflo(hli[11]); PIN(ub); SBAR();
    SBAR();
    zi0 = FMA(pfl[0], zi1, t3_); PIN(zi0); SBAR(); zr0 = FMA(pfl[0], zr1, -t2_); PIN(zr0); SBAR();
    SBAR();
    bcc[5] = MFMA(wf7, hs[3], bcc[5]);
    SBAR();
    wf9 = ldb128(rb1 + 16384);
    SBAR();
    ui0 = FMA(-pfg[1], ua, -zi0); PIN(ui0); SBAR(); ur0 = FMA(-pfg[1], ub, zr0); PIN(ur0); SBAR();
    SBAR();
    ui0 = FMA(-pfg[0], ub, ui0); PIN(ui0); SBAR(); ur0 = FMA(pfg[0], ua, ur0); PIN(ur0); SBAR();
    SBAR();
    t2_ = pfl[1] * zi0; PIN(t2_); SBAR(); t3_ = pfl[1] * zr0; PIN(t3_); SBAR(); ua = bfhi(hlr[11]); PIN(ua); SBAR(); ub = bfhi(hli[11]); PIN(ub); SBAR();
    SBAR();
    bcc[5] = MFMA(wf8, hs[4], bcc[5]);
    SBAR();
    wf10 = ldb128(rb2 + 16384);
    SBAR();
    zi1 = FMA(pfl[0], zi0, t3_); PIN(zi1); SBAR(); zr1 = FMA(pfl[0], zr0, -t2_); PIN(zr1); SBAR();
    SBAR();
    ui1 = FMA(-pfg[1], ua, -zi1); PIN(ui1); SBAR(); ur1 = FMA(-pfg[1], ub, zr1); PIN(ur1); SBAR();
    SBAR();
    bcc[5] = MFMA(wf9, hs[5], bcc[5]);
    SBAR();
    wf11 = ldb128(rb3 + 16384);
    SBAR();
    ui1 = FMA(-pfg[0], ub, ui1); PIN(ui1); SBAR(); ur1 = FMA(pfg[0], ua, ur1); PIN(ur1); SBAR(); oi3 = cvtpk(ui0, ui1); PIN(oi3); SBAR(); or3 = cvtpk(ur0, ur1); PIN(or3); SBAR(); { const lptr p_ = HW(0); stu4(p_ + 4096, (u32x4){oi0, oi1, oi2, oi3}); stu4(p_, (u32x4){or0, or1, or2, or3}); }
    SBAR();
    t2_ = pfl[1] * zi1; PIN(t2_); SBAR(); t3_ = pfl[1] * zr1; PIN(t3_); SBAR(); ua = bflo(hlr[12]); PIN(ua); SBAR(); ub = bflo(hli[12]); PIN(ub); SBAR();
    SBAR();
    zi0 = FMA(pfl[0], zi1, t3_); PIN(zi0); SBAR(); zr0 = FMA(pfl[0], zr1, -t2_); PIN(zr0); SBAR();
    SBAR();
    GL(gci, ROT9(2) * 73728 + 128, voffR, 90112);
    SBAR();
    bcc[5] = MFMA(wf10, hs[6], bcc[5]);
    SBAR();
    rb0 += -98304; PIN(rb0); wf0 = ldb128(rb0 + 0);
    rb1 += -98304; PIN(rb1); wf1 = ldb128(rb1 + 0);
    SBAR();
    ui0 = FMA(-pfg[1], ua, -zi0); PIN(ui0); SBAR(); ur0 = FMA(-pfg[1], ub, zr0); PIN(ur0); SBAR();
    SBAR();
    ui0 = FMA(-pfg[0], ub, ui0); PIN(ui0); SBAR(); ur0 = FMA(pfg[0], ua, ur0); PIN(ur0); SBAR();
    SBAR();
    bcc[5] = MFMA(wf11, hs[7], bcc[5]);
    SBAR();
    t2_ = pfl[1] * zi0; PIN(t2_); SBAR(); t3_ = pfl[1] * zr0; PIN(t3_); SBAR(); ua = bfhi(hlr[12]); PIN(ua); SBAR(); ub = bfhi(hli[12]); PIN(ub); SBAR();
    SBAR();
    zi1 = FMA(pfl[0], zi0, t3_); PIN(zi1); SBAR(); zr1 = FMA(pfl[0], zr0, -t2_); PIN(zr1); SBAR();
    SBAR();
    ui1 = FMA(-pfg[1], ua, -zi1); PIN(ui1); SBAR(); ur1 = FMA(-pfg[1], ub, zr1); PIN(ur1); SBAR();
    SBAR();
    asm volatile("s_waitcnt vmcnt(6)\n\ts_barrier" ::: "memory"); SBAR();
    bcc[6] = MFMA(wf0, hs[0], zero16);
    SBAR();
    rb2 += -98304; PIN(rb2); wf2 = ldb128(rb2 + 0);
    SBAR();
    ui1 = FMA(-pfg[0], ub, ui1); PIN(ui1); SBAR(); ur1 = FMA(pfg[0], ua, ur1); PIN(ur1); SBAR(); oi0 = cvtpk(ui0, ui1); PIN(oi0); SBAR(); or0 = cvtpk(ur0, ur1); PIN(or0); SBAR();
    SBAR();
    t2_ = pfl[1] * zi1; PIN(t2_); SBAR(); t3_ = pfl[1] * zr1; PIN(t3_); SBAR(); ua = bflo(hlr[13]); PIN(ua); SBAR(); ub = bflo(hli[13]); PIN(ub); SBAR();
    SBAR();
    zi0 = FMA(pfl[0], zi1, t3_); PIN(zi0); SBAR(); zr0 = FMA(pfl[0], zr1, -t2_); PIN(zr0); SBAR();
    SBAR();
    bcc[6] = MFMA(wf1, hs[1], bcc[6]);
    SBAR();
    rb3 += -98304; PIN(rb3); wf3 = ldb128(rb3 + 0);
    SBAR();
    ui0 = FMA(-pfg[1], ua, -zi0); PIN(ui0); SBAR(); ur0 = FMA(-pfg[1], ub, zr0); PIN(ur0); SBAR();
    SBAR();
    ui0 = FMA(-pfg[0], ub, ui0); PIN(ui0); SBAR(); ur0 = FMA(pfg[0], ua, ur0); PIN(ur0); SBAR();
    SBAR();
    GL(gcr, ROT9(3) * 73728 + 128, voffR, 98304);
    SBAR();
    bcc[6] = MFMA(wf2, hs[2], bcc[6]);
    SBAR();
    wf4 = ldb128(rb0 + 8192);
    SBAR();
    t2_ = pfl[1] * zi0; PIN(t2_); SBAR(); t3_ = pfl[1] * zr0; PIN(t3_); SBAR(); ua = bfhi(hlr[13]); PIN(ua); SBAR(); ub = bfhi(hli[13]); PIN(ub); SBAR();
    SBAR();
    zi1 = FMA(pfl[0], zi0, t3_); PIN(zi1); SBAR(); zr1 = FMA(pfl[0], zr0, -t2_); PIN(zr1); SBAR();
    SBAR();
    ui1 = FMA(-pfg[1], ua, -zi1); PIN(ui1); SBAR(); ur1 = FMA(-pfg[1], ub, zr1); PIN(ur1); SBAR();
    SBAR();
    bcc[6] = MFMA(wf3, hs[3], bcc[6]);
    SBAR();
    wf5 = ldb128(rb1 + 8192);
    SBAR();
    ui1 = FMA(-pfg[0], ub, ui1); PIN(ui1); SBAR(); ur1 = FMA(pfg[0], ua, ur1); PIN(ur1); SBAR(); oi1 = cvtpk(ui0, ui1); PIN(oi1); SBAR(); or1 = cvtpk(ur0, ur1); PIN(or1); SBAR();
    SBAR();
    t2_ = pfl[1] * zi1; PIN(t2_); SBAR(); t3_ = pfl[1] * zr1; PIN(t3_); SBAR(); ua = bflo(hlr[14]); PIN(ua); SBAR(); ub = bflo(hli[14]); PIN(ub); SBAR();
    SBAR();
    bcc[6] = MFMA(wf4, hs[4], bcc[6]);
    SBAR();
    wf6 = ldb128(rb2 + 8192);
    SBAR();
    zi0 = FMA(pfl[0], zi1, t3_); PIN(zi0); SBAR(); zr0 = FMA(pfl[0], zr1, -t2_); PIN(zr0); SBAR();
    SBAR();
    ui0 = FMA(-pfg[1], ua, -zi0); PIN(ui0); SBAR(); ur0 = FMA(-pfg[1], ub, zr0); PIN(ur0); SBAR();
    SBAR();
    ui0 = FMA(-pfg[0], ub, ui0); PIN(ui0); SBAR(); ur0 = FMA(pfg[0], ua, ur0); PIN(ur0); SBAR();
    SBAR();
    bcc[6] = MFMA(wf5, hs[5], bcc[6]);
    SBAR();
    wf7 = ldb128(rb3 + 8192);
    SBAR();
    t2_ = pfl[1] * zi0; PIN(t2_); SBAR(); t3_ = pfl[1] * zr0; PIN(t3_); SBAR(); ua = bfhi(hlr[14]); PIN(ua); SBAR(); ub = bfhi(hli[14]); PIN(ub); SBAR();
    SBAR();
    zi1 = FMA(pfl[0], zi0, t3_); PIN(zi1); SBAR(); zr1 = FMA(pfl[0], zr0, -t2_); PIN(zr1); SBAR();
    SBAR();
    GL(gci, ROT9(3) * 73728 + 128, voffR, 106496);
    SBAR();
    bcc[6] = MFMA(wf6, hs[6], bcc[6]);
    SBAR();
    wf8 = ldb128(rb0 + 16384);
    SBAR();
    ui1 = FMA(-pfg[1], ua, -zi1); PIN(ui1); SBAR(); ur1 = FMA(-pfg[1], ub, zr1); PIN(ur1); SBAR();
    SBAR();
    ui1 = FMA(-pfg[0], ub, ui1); PIN(ui1); SBAR(); ur1 = FMA(pfg[0], ua, ur1); PIN(ur1); SBAR(); oi2 = cvtpk(ui0, ui1); PIN(oi2); SBAR(); or2 = cvtpk(ur0, ur1); PIN(or2); SBAR();
    SBAR();
    t2_ = pfl[1] * zi1; PIN(t2_); SBAR(); t3_ = pfl[1] * zr1; PIN(t3_); SBAR(); ua = bflo(hlr[15]); PIN(ua); SBAR(); ub = bflo(hli[15]); PIN(ub); SBAR();
    SBAR();
    bcc[6] = MFMA(wf7, hs[7], bcc[6]);
    SBAR();
    wf9 = ldb128(rb1 + 16384);
    SBAR();
    zi0 = FMA(pfl[0], zi1, t3_); PIN(zi0); SBAR(); zr0 = FMA(pfl[0], zr1, -t2_); PIN(zr0); SBAR();
    SBAR();
    ui0 = FMA(-pfg[1], ua, -zi0); PIN(ui0); SBAR(); ur0 = FMA(-pfg[1], ub, zr0); PIN(ur0); SBAR();
    SBAR();
    bcc[7] = MFMA(wf8, hs[0], zero16);
    SBAR();
    wf10 = ldb128(rb2 + 16384);
    SBAR();
    ui0 = FMA(-pfg[0], ub, ui0); PIN(ui0); SBAR(); ur0 = FMA(pfg[0], ua, ur0); PIN(ur0); SBAR();
    SBAR();
    t2_ = pfl[1] * zi0; PIN(t2_); SBAR(); t3_ = pfl[1] * zr0; PIN(t3_); SBAR(); ua = bfhi(hlr[15]); PIN(ua); SBAR(); ub = bfhi(hli[15]); PIN(ub); SBAR();
    SBAR();
    zi1 = FMA(pfl[0], zi0, t3_); PIN(zi1); SBAR(); zr1 = FMA(pfl[0], zr0, -t2_); PIN(zr1); SBAR();
    SBAR();
    bcc[7] = MFMA(wf9, hs[1], bcc[7]);
    SBAR();
    wf11 = ldb128(rb3 + 16384);
    SBAR();
    ui1 = FMA(-pfg[1], ua, -zi1); PIN(ui1); SBAR(); ur1 = FMA(-pfg[1], ub, zr1); PIN(ur1); SBAR();
    SBAR();
    ui1 = FMA(-pfg[0], ub, ui1); PIN(ui1); SBAR(); ur1 = FMA(pfg[0], ua, ur1); PIN(ur1); SBAR(); oi3 = cvtpk(ui0, ui1); PIN(oi3); SBAR(); or3 = cvtpk(ur0, ur1); PIN(or3); SBAR(); { const lptr p_ = HW(1); stu4(p_ + 4096, (u32x4){oi0, oi1, oi2, oi3}); stu4(p_, (u32x4){or0, or1, or2, or3}); }
    SBAR();
    GL(gcr, ROT9(4) * 73728 + 128, voffR, 114688);
    SBAR();
    bcc[7] = MFMA(wf10, hs[2], bcc[7]);
    SBAR();
    wf0 = ldb128(rb0 + 24576);
    wf1 = ldb128(rb1 + 24576);
    SBAR();
    bcc[7] = MFMA(wf11, hs[3], bcc[7]);
    SBAR();
    asm volatile("s_waitcnt vmcnt(6) lgkmcnt(2)\n\ts_barrier" ::: "memory"); SBAR();
    bcc[7] = MFMA(wf0, hs[4], bcc[7]);
    SBAR();
    wf2 = ldb128(rb2 + 24576);
    SBAR();
    LB_ADDR(); ALD4(g0, lbo0, ggr + 32768); ALD4(g1, lbo1, ggr + 32768); ALD4(tc, voffC, gpr + 8192);
    SBAR();
    bcc[7] = MFMA(wf1, hs[5], bcc[7]);
    SBAR();
    wf3 = ldb128(rb3 + 24576);
    SBAR();
    hs[8] = trfrag2(ht0 + 0, ht1 + 0);
    SBAR();
    GL(gci, ROT9(4) * 73728 + 128, voffR, 0);
    SBAR();
    bcc[7] = MFMA(wf2, hs[6], bcc[7]);
    SBAR();
    wf4 = ldb128(rb0 + 32768);
    SBAR();
    hs[9] = trfrag2(ht0 + 1024, ht1 + 1024);
    SBAR();
    bcc[7] = MFMA(wf3, hs[7], bcc[7]);
    SBAR();
    wf5 = ldb128(rb1 + 32768);
    SBAR();
    hs[10] = trfrag2(ht0 + 2048, ht1 + 2048);
    SBAR();
    bcc[8] = MFMA(wf4, hs[0], zero16);
    SBAR();
    wf6 = ldb128(rb2 + 32768);
    SBAR();
    hs[11] = trfrag2(ht0 + 3072, ht1 + 3072);
    SBAR();
    bcc[8] = MFMA(wf5, hs[1], bcc[8]);
    SBAR();
    wf7 = ldb128(rb3 + 32768);
    SBAR();
    hs[12] = trfrag2(ht0 + 4096, ht1 + 4096);
    SBAR();
    GL(gcr, ROT9(5) * 73728 + 128, voffR, 8192);
    SBAR();
    bcc[8] = MFMA(wf6, hs[2], bcc[8]);
    SBAR();
    wf8 = ldb128(rb0 + 40960);
    SBAR();
    hs[13] = trfrag2(ht0 + 5120, ht1 + 5120);
    SBAR();
    bcc[8] = MFMA(wf7, hs[3], bcc[8]);
    SBAR();
    wf9 = ldb128(rb1 + 40960);
    SBAR();
    hs[14] = trfrag2(ht0 + 6144, ht1 + 6144);
    SBAR();
    bcc[8] = MFMA(wf8, hs[4], bcc[8]);
    SBAR();
    wf10 = ldb128(rb2 + 40960);
    SBAR();
    hs[15] = trfrag2(ht0 + 7168, ht1 + 7168);
    SBAR();
    bcc[8] = MFMA(wf9, hs[5], bcc[8]);
    SBAR();
    wf11 = ldb128(rb3 + 40960);
    SBAR();
    GL(gci, ROT9(5) * 73728 + 128, voffR, 16384);
    SBAR();
    bcc[8] = MFMA(wf10, hs[6], bcc[8]);
    SBAR();
    rb0 += 49152; PIN(rb0); wf0 = ldb128(rb0 + 0);
    rb1 += 49152; PIN(rb1); wf1 = ldb128(rb1 + 0);
    SBAR();
    bcc[8] = MFMA(wf11, hs[7], bcc[8]);
    SBAR();
    asm volatile("s_waitcnt vmcnt(9)\n\ts_barrier" ::: "memory"); SBAR();
    bcc[0] = MFMA(wf0, hs[8], bcc[0]);
    SBAR();
    rb2 += 49152; PIN(rb2); wf2 = ldb128(rb2 + 0);
    SBAR();
    bcc[0] = MFMA(wf1, hs[9], bcc[0]);
    SBAR();
    rb3 += 49152; PIN(rb3); wf3 = ldb128(rb3 + 0);
    SBAR();
    VWAIT3(3, g0, g1, tc); LB_CHECK(2);
    SBAR();
    GL(gcr, ROT9(6) * 73728 + 128, voffR, 24576);
    SBAR();
    bcc[0] = MFMA(wf2, hs[10], bcc[0]);
    SBAR();
    wf4 = ldb128(rb0 + 8192);
    SBAR();
    t0_ = FMA(-TBF(tc, 1), TBF(g1, 2), TBF(g0, 0)); PIN(t0_); SBAR(); t1_ = FMA(TBF(tc, 1), TBF(g1, 0), TBF(g0, 2)); PIN(t1_); SBAR(); t0_ = FMA(TBF(tc, 0), TBF(g1, 0), t0_); PIN(t0_); SBAR(); t1_ = FMA(TBF(tc, 0), TBF(g1, 2), t1_); PIN(t1_); SBAR();
    SBAR();
    bcc[0] = MFMA(wf3, hs[11], bcc[0]);
    SBAR();
    wf5 = ldb128(rb1 + 8192);
    SBAR();
    stf2(pw, t0_, t1_);
    SBAR();
    bcc[0] = MFMA(wf4, hs[12], bcc[0]);
    SBAR();
    wf6 = ldb128(rb2 + 8192);
    SBAR();
    bcc[0] = MFMA(wf5, hs[13], bcc[0]);
    SBAR();
    wf7 = ldb128(rb3 + 8192);
    SBAR();
    GL(gci, ROT9(6) * 73728 + 128, voffR, 32768);
    SBAR();
    bcc[0] = MFMA(wf6, hs[14], bcc[0]);
    SBAR();
    wf8 = ldb128(rb0 + 16384);
    SBAR();
    bcc[0] = MFMA(wf7, hs[15], bcc[0]);
    SBAR();
    wf9 = ldb128(rb1 + 16384);
    SBAR();
    bcc[1] = MFMA(wf8, hs[8], bcc[1]);
    SBAR();
    wf10 = ldb128(rb2 + 16384);
    SBAR();
    bcc[1] = MFMA(wf9, hs[9], bcc[1]);
    SBAR();
    wf11 = ldb128(rb3 + 16384);
    SBAR();
    GL(gcr, ROT9(7) * 73728 + 128, voffR, 40960);
    SBAR();
    bcc[1] = MFMA(wf10, hs[10], bcc[1]);
    SBAR();
    wf0 = ldb128(rb0 + 24576);
    wf1 = ldb128(rb1 + 24576);
    SBAR();
    bcc[1] = MFMA(wf11, hs[11], bcc[1]);
    SBAR();
    asm volatile("s_waitcnt vmcnt(9) lgkmcnt(2)\n\ts_barrier" ::: "memory"); SBAR();
    bcc[1] = MFMA(wf0, hs[12], bcc[1]);
    SBAR();
    wf2 = ldb128(rb2 + 24576);
    SBAR();
    fa0 = ldf2(sq + 3584); fb0 = ldf2(sq + 3072);
    SBAR();
    bcc[1] = MFMA(wf1, hs[13], bcc[1]);
    SBAR();
    wf3 = ldb128(rb3 + 24576);
    SBAR();
    fa1 = ldf2(sq + 2560); fb1 = ldf2(sq + 2048);
    SBAR();
    GL(gci, ROT9(7) * 73728 + 128, voffR, 49152);
    SBAR();
    bcc[1] = MFMA(wf2, hs[14], bcc[1]);
    SBAR();
    wf4 = ldb128(rb0 + 32768);
    SBAR();
    Cr = fa0.x; Ci = fa0.y;
    SBAR();
    bcc[1] = MFMA(wf3, hs[15], bcc[1]);
    SBAR();
    wf5 = ldb128(rb1 + 32768);
    SBAR();
    t2_ = FMA(-TBF(tc, 3), Ci, fb0.x); PIN(t2_); SBAR(); t3_ = FMA(TBF(tc, 3), Cr, fb0.y); PIN(t3_); SBAR(); Cr = FMA(TBF(tc, 2), Cr, t2_); PIN(Cr); SBAR(); Ci = FMA(TBF(tc, 2), Ci, t3_); PIN(Ci); SBAR();
    SBAR();
    bcc[2] = MFMA(wf4, hs[8], bcc[2]);
    SBAR();
    wf6 = ldb128(rb2 + 32768);
    SBAR();
    t2_ = FMA(-TBF(tc, 3), Ci, fa1.x); PIN(t2_); SBAR(); t3_ = FMA(TBF(tc, 3), Cr, fa1.y); PIN(t3_); SBAR(); Cr = FMA(TBF(tc, 2), Cr, t2_); PIN(Cr); SBAR(); Ci = FMA(TBF(tc, 2), Ci, t3_); PIN(Ci); SBAR();
    SBAR();
    fa0 = ldf2(sq + 1536); fb0 = ldf2(sq + 1024);
    SBAR();
    bcc[2] = MFMA(wf5, hs[9], bcc[2]);
    SBAR();
    wf7 = ldb128(rb3 + 32768);
    SBAR();
    t2_ = FMA(-TBF(tc, 3), Ci, fb1.x); PIN(t2_); SBAR(); t3_ = FMA(TBF(tc, 3), Cr, fb1.y); PIN(t3_); SBAR(); Cr = FMA(TBF(tc, 2), Cr, t2_); PIN(Cr); SBAR(); Ci = FMA(TBF(tc, 2), Ci, t3_); PIN(Ci); SBAR();
    SBAR();
    GL(gcr, ROT9(8) * 73728 + 128, voffR, 57344);
    SBAR();
    bcc[2] = MFMA(wf6, hs[10], bcc[2]);
    SBAR();
    wf8 = ldb128(rb0 + 40960);
    SBAR();
    t2_ = FMA(-TBF(tc, 3), Ci, fa0.x); PIN(t2_); SBAR(); t3_ = FMA(TBF(tc, 3), Cr, fa0.y); PIN(t3_); SBAR(); Cr = FMA(TBF(tc, 2), Cr, t2_); PIN(Cr); SBAR(); Ci = FMA(TBF(tc, 2), Ci, t3_); PIN(Ci); SBAR();
    SBAR();
    fa1 = ldf2(sq + 512); fb1 = ldf2(sq + 0);
    SBAR();
    bcc[2] = MFMA(wf7, hs[11], bcc[2]);
    SBAR();
    wf9 = ldb128(rb1 + 40960);
    SBAR();
    t2_ = FMA(-TBF(tc, 3), Ci, fb0.x); PIN(t2_); SBAR(); t3_ = FMA(TBF(tc, 3), Cr, fb0.y); PIN(t3_); SBAR(); Cr = FMA(TBF(tc, 2), Cr, t2_); PIN(Cr); SBAR(); Ci = FMA(TBF(tc, 2), Ci, t3_); PIN(Ci); SBAR();
    SBAR();
    bcc[2] = MFMA(wf8, hs[12], bcc[2]);
    SBAR();
    wf10 = ldb128(rb2 + 40960);
    SBAR();
    t2_ = FMA(-TBF(tc, 3), Ci, fa1.x); PIN(t2_); SBAR(); t3_ = FMA(TBF(tc, 3), Cr, fa1.y); PIN(t3_); SBAR(); Cr = FMA(TBF(tc, 2), Cr, t2_); PIN(Cr); SBAR(); Ci = FMA(TBF(tc, 2), Ci, t3_); PIN(Ci); SBAR();
    SBAR();
    pfl = ldf2(sq + 38400); pfg = ldf2(sq + 39936);
    SBAR();
    bcc[2] = MFMA(wf9, hs[13], bcc[2]);
    SBAR();
    wf11 = ldb128(rb3 + 40960);
    SBAR();
    t2_ = FMA(-TBF(tc, 3), Ci, fb1.x); PIN(t2_); SBAR(); t3_ = FMA(TBF(tc, 3), Cr, fb1.y); PIN(t3_); SBAR(); Cr = FMA(TBF(tc, 2), Cr, t2_); PIN(Cr); SBAR(); Ci = FMA(TBF(tc, 2), Ci, t3_); PIN(Ci); SBAR();
    SBAR();
    GL(gci, ROT9(8) * 73728 + 128, voffR, 65536);
    SBAR();
    bcc[2] = MFMA(wf10, hs[14], bcc[2]);
    SBAR();
    rb0 += 49152; PIN(rb0); wf0 = ldb128(rb0 + 0);
    rb1 += 49152; PIN(rb1); wf1 = ldb128(rb1 + 0);
    SBAR();
    t2_ = FMA(-TBF(tb0, 3), Ci, Pwr); PIN(t2_); SBAR(); t3_ = FMA(TBF(tb0, 3), Cr, Pwi); PIN(t3_); SBAR(); Cr = FMA(TBF(tb0, 2), Cr, t2_); PIN(Cr); SBAR(); Ci = FMA(TBF(tb0, 2), Ci, t3_); PIN(Ci); SBAR();
    SBAR();
    bcc[2] = MFMA(wf11, hs[15], bcc[2]);
    SBAR();
    asm volatile("s_waitcnt vmcnt(6)\n\ts_barrier" ::: "memory"); SBAR();
    bcc[3] = MFMA(wf0, hs[8], bcc[3]);
    SBAR();
    rb2 += 49152; PIN(rb2); wf2 = ldb128(rb2 + 0);
    SBAR();
    t2_ = pfg[1] * Ci; PIN(t2_); SBAR(); t3_ = pfg[1] * Cr; PIN(t3_); SBAR(); zr1 = FMA(pfg[0], Cr, -t2_); PIN(zr1); SBAR(); zi1 = FMA(pfg[0], Ci, t3_); PIN(zi1); SBAR();
    SBAR();
    bcc[3] = MFMA(wf1, hs[9], bcc[3]);
    SBAR();
    rb3 += 49152; PIN(rb3); wf3 = ldb128(rb3 + 0);
    SBAR();
    GL(gcr, ROT9(0) * 73728 + 256, voffR, 73728);
    SBAR();
    bcc[3] = MFMA(wf2, hs[10], bcc[3]);
    SBAR();
    wf4 = ldb128(rb0 + 8192);
    SBAR();
    t2_ = pfl[1] * zi1; PIN(t2_); SBAR(); t3_ = pfl[1] * zr1; PIN(t3_); SBAR(); ua = bflo(hlr[0]); PIN(ua); SBAR(); ub = bflo(hli[0]); PIN(ub); SBAR();
    SBAR();
    zi0 = FMA(pfl[0], zi1, t3_); PIN(zi0); SBAR(); zr0 = FMA(pfl[0], zr1, -t2_); PIN(zr0); SBAR();
    SBAR();
    bcc[3] = MFMA(wf3, hs[11], bcc[3]);
    SBAR();
    wf5 = ldb128(rb1 + 8192);
    SBAR();
    ui0 = FMA(-pfg[1], ua, -zi0); PIN(ui0); SBAR(); ur0 = FMA(-pfg[1], ub, zr0); PIN(ur0); SBAR();
    SBAR();
    ui0 = FMA(-pfg[0], ub, ui0); PIN(ui0); SBAR(); ur0 = FMA(pfg[0], ua, ur0); PIN(ur0); SBAR();
    SBAR();
    bcc[3] = MFMA(wf4, hs[12], bcc[3]);
    SBAR();
    wf6 = ldb128(rb2 + 8192);
    SBAR();
    t2_ = pfl[1] * zi0; PIN(t2_); SBAR(); t3_ = pfl[1] * zr0; PIN(t3_); SBAR(); ua = bfhi(hlr[0]); PIN(ua); SBAR(); ub = bfhi(hli[0]); PIN(ub); SBAR();
    SBAR();
    zi1 = FMA(pfl[0], zi0, t3_); PIN(zi1); SBAR(); zr1 = FMA(pfl[0], zr0, -t2_); PIN(zr1); SBAR();
    SBAR();
    bcc[3] = MFMA(wf5, hs[13], bcc[3]);
    SBAR();
    wf7 = ldb128(rb3 + 8192);
    SBAR();
    ui1 = FMA(-pfg[1], ua, -zi1); PIN(ui1); SBAR(); ur1 = FMA(-pfg[1], ub, zr1); PIN(ur1); SBAR();
    SBAR();
    ui1 = FMA(-pfg[0], ub, ui1); PIN(ui1); SBAR(); ur1 = FMA(pfg[0], ua, ur1); PIN(ur1); SBAR(); oi0 = cvtpk(ui0, ui1); PIN(oi0); SBAR(); or0 = cvtpk(ur0, ur1); PIN(or0); SBAR();
    SBAR();
    GL(gci, ROT9(0) * 73728 + 256, voffR, 81920);
    SBAR();
    bcc[3] = MFMA(wf6, hs[14], bcc[3]);
    SBAR();
    wf8 = ldb128(rb0 + 16384);
    SBAR();
    t2_ = pfl[1] * zi1; PIN(t2_); SBAR(); t3_ = pfl[1] * zr1; PIN(t3_); SBAR(); ua = bflo(hlr[1]); PIN(ua); SBAR(); ub = bflo(hli[1]); PIN(ub); SBAR();
    SBAR();
    zi0 = FMA(pfl[0], zi1, t3_); PIN(zi0); SBAR(); zr0 = FMA(pfl[0], zr1, -t2_); PIN(zr0); SBAR();
    SBAR();
    bcc[3] = MFMA(wf7, hs[15], bcc[3]);
    SBAR();
    wf9 = ldb128(rb1 + 16384);
    SBAR();
    ui0 = FMA(-pfg[1], ua, -zi0); PIN(ui0); SBAR(); ur0 = FMA(-pfg[1], ub, zr0); PIN(ur0); SBAR();
    SBAR();
    ui0 = FMA(-pfg[0], ub, ui0); PIN(ui0); SBAR(); ur0 = FMA(pfg[0], ua, ur0); PIN(ur0); SBAR();
    SBAR();
    bcc[4] = MFMA(wf8, hs[8], bcc[4]);
    SBAR();
    wf10 = ldb128(rb2 + 16384);
    SBAR();
    t2_ = pfl[1] * zi0; PIN(t2_); SBAR(); t3_ = pfl[1] * zr0; PIN(t3_); SBAR(); ua = bfhi(hlr[1]); PIN(ua); SBAR(); ub = bfhi(hli[1]); PIN(ub); SBAR();
    SBAR();
    zi1 = FMA(pfl[0], zi0, t3_); PIN(zi1); SBAR(); zr1 = FMA(pfl[0], zr0, -t2_); PIN(zr1); SBAR();
    SBAR();
    bcc[4] = MFMA(wf9, hs[9], bcc[4]);
    SBAR();
    wf11 = ldb128(rb3 + 16384);
    SBAR();
    ui1 = FMA(-pfg[1], ua, -zi1); PIN(ui1); SBAR(); ur1 = FMA(-pfg[1], ub, zr1); PIN(ur1); SBAR();
    SBAR();
    ui1 = FMA(-pfg[0], ub, ui1); PIN(ui1); SBAR(); ur1 = FMA(pfg[0], ua, ur1); PIN(ur1); SBAR(); oi1 = cvtpk(ui0, ui1); PIN(oi1); SBAR(); or1 = cvtpk(ur0, ur1); PIN(or1); SBAR();
    SBAR();
    GL(gcr, ROT9(1) * 73728 + 256, voffR, 90112);
    SBAR();
    bcc[4] = MFMA(wf10, hs[10], bcc[4]);
    SBAR();
    rb0 += -98304; PIN(rb0); wf0 = ldb128(rb0 + 0);
    rb1 += -98304; PIN(rb1); wf1 = ldb128(rb1 + 0);
    SBAR();
    t2_ = pfl[1] * zi1; PIN(t2_); SBAR(); t3_ = pfl[1] * zr1; PIN(t3_); SBAR(); ua = bflo(hlr[2]); PIN(ua); SBAR(); ub = bflo(hli[2]); PIN(ub); SBAR();
    SBAR();
    zi0 = FMA(pfl[0], zi1, t3_); PIN(zi0); SBAR(); zr0 = FMA(pfl[0], zr1, -t2_); PIN(zr0); SBAR();
    SBAR();
    bcc[4] = MFMA(wf11, hs[11], bcc[4]);
    SBAR();
    ui0 = FMA(-pfg[1], ua, -zi0); PIN(ui0); SBAR(); ur0 = FMA(-pfg[1], ub, zr0); PIN(ur0); SBAR();
    SBAR();
    ui0 = FMA(-pfg[0], ub, ui0); PIN(ui0); SBAR(); ur0 = FMA(pfg[0], ua, ur0); PIN(ur0); SBAR();
    SBAR();
    asm volatile("s_waitcnt vmcnt(6)\n\ts_barrier" ::: "memory"); SBAR();
    bcc[4] = MFMA(wf0, hs[12], bcc[4]);
    SBAR();
    rb2 += -98304; PIN(rb2); wf2 = ldb128(rb2 + 0);
    SBAR();
    t2_ = pfl[1] * zi0; PIN(t2_); SBAR(); t3_ = pfl[1] * zr0; PIN(t3_); SBAR(); ua = bfhi(hlr[2]); PIN(ua); SBAR(); ub = bfhi(hli[2]); PIN(ub); SBAR();
    SBAR();
    zi1 = FMA(pfl[0], zi0, t3_); PIN(zi1); SBAR(); zr1 = FMA(pfl[0], zr0, -t2_); PIN(zr1); SBAR();
    SBAR();
    bcc[4] = MFMA(wf1, hs[13], bcc[4]);
    SBAR();
    rb3 += -98304; PIN(rb3); wf3 = ldb128(rb3 + 0);
    SBAR();
    ui1 = FMA(-pfg[1], ua, -zi1); PIN(ui1); SBAR(); ur1 = FMA(-pfg[1], ub, zr1); PIN(ur1); SBAR();
    SBAR();
    ui1 = FMA(-pfg[0], ub, ui1); PIN(ui1); SBAR(); ur1 = FMA(pfg[0], ua, ur1); PIN(ur1); SBAR(); oi2 = cvtpk(ui0, ui1); PIN(oi2); SBAR(); or2 = cvtpk(ur0, ur1); PIN(or2); SBAR();
    SBAR();
    GL(gci, ROT9(1) * 73728 + 256, voffR, 98304);
    SBAR();
    bcc[4] = MFMA(wf2, hs[14], bcc[4]);
    SBAR();
    wf4 = ldb128(rb0 + 8192);
    SBAR();
    t2_ = pfl[1] * zi1; PIN(t2_); SBAR(); t3_ = pfl[1] * zr1; PIN(t3_); SBAR(); ua = bflo(hlr[3]); PIN(ua); SBAR(); ub = bflo(hli[3]); PIN(ub); SBAR();
    SBAR();
    zi0 = FMA(pfl[0], zi1, t3_); PIN(zi0); SBAR(); zr0 = FMA(pfl[0], zr1, -t2_); PIN(zr0); SBAR();
    SBAR();
    bcc[4] = MFMA(wf3, hs[15], bcc[4]);
    SBAR();
    wf5 = ldb128(rb1 + 8192);
    SBAR();
    ui0 = FMA(-pfg[1], ua, -zi0); PIN(ui0); SBAR(); ur0 = FMA(-pfg[1], ub, zr0); PIN(ur0); SBAR();
    SBAR();
    ui0 = FMA(-pfg[0], ub, ui0); PIN(ui0); SBAR(); ur0 = FMA(pfg[0], ua, ur0); PIN(ur0); SBAR();
    SBAR();
    bcc[5] = MFMA(wf4, hs[8], bcc[5]);
    SBAR();
    wf6 = ldb128(rb2 + 8192);
    SBAR();
    t2_ = pfl[1] * zi0; PIN(t2_); SBAR(); t3_ = pfl[1] * zr0; PIN(t3_); SBAR(); ua = bfhi(hlr[3]); PIN(ua); SBAR(); ub = bfhi(hli[3]); PIN(ub); SBAR();
    SBAR();
    zi1 = FMA(pfl[0], zi0, t3_); PIN(zi1); SBAR(); zr1 = FMA(pfl[0], zr0, -t2_); PIN(zr1); SBAR();
    SBAR();
    bcc[5] = MFMA(wf5, hs[9], bcc[5]);
    SBAR();
    wf7 = ldb128(rb3 + 8192);
    SBAR();
    ui1 = FMA(-pfg[1], ua, -zi1); PIN(ui1); SBAR(); ur1 = FMA(-pfg[1], ub, zr1); PIN(ur1); SBAR();
    SBAR();
    ui1 = FMA(-pfg[0], ub, ui1); PIN(ui1); SBAR(); ur1 = FMA(pfg[0], ua, ur1); PIN(ur1); SBAR(); oi3 = cvtpk(ui0, ui1); PIN(oi3); SBAR(); or3 = cvtpk(ur0, ur1); PIN(or3); SBAR(); { const lptr p_ = HW(0); stu4(p_ + 4096, (u32x4){oi0, oi1, oi2, oi3}); stu4(p_, (u32x4){or0, or1, or2, or3}); }
    SBAR();
    GL(gcr, ROT9(2) * 73728 + 256, voffR, 106496);
    SBAR();
    bcc[5] = MFMA(wf6, hs[10], bcc[5]);
    SBAR();
    wf8 = ldb128(rb0 + 16384);
    SBAR();
    t2_ = pfl[1] * zi1; PIN(t2_); SBAR(); t3_ = pfl[1] * zr1; PIN(t3_); SBAR(); ua = bflo(hlr[4]); PIN(ua); SBAR(); ub = bflo(hli[4]); PIN(ub); SBAR();
    SBAR();
    zi0 = FMA(pfl[0], zi1, t3_); PIN(zi0); SBAR(); zr0 = FMA(pfl[0], zr1, -t2_); PIN(zr0); SBAR();
    SBAR();
    bcc[5] = MFMA(wf7, hs[11], bcc[5]);
    SBAR();
    wf9 = ldb128(rb1 + 16384);
    SBAR();
    ui0 = FMA(-pfg[1], ua, -zi0); PIN(ui0); SBAR(); ur0 = FMA(-pfg[1], ub, zr0); PIN(ur0); SBAR();
    SBAR();
    ui0 = FMA(-pfg[0], ub, ui0); PIN(ui0); SBAR(); ur0 = FMA(pfg[0], ua, ur0); PIN(ur0); SBAR();
    SBAR();
    bcc[5] = MFMA(wf8, hs[12], bcc[5]);
    SBAR();
    wf10 = ldb128(rb2 + 16384);
    SBAR();
    t2_ = pfl[1] * zi0; PIN(t2_); SBAR(); t3_ = pfl[1] * zr0; PIN(t3_); SBAR(); ua = bfhi(hlr[4]); PIN(ua); SBAR(); ub = bfhi(hli[4]); PIN(ub); SBAR();
    SBAR();
    zi1 = FMA(pfl[0], zi0, t3_); PIN(zi1); SBAR(); zr1 = FMA(pfl[0], zr0, -t2_); PIN(zr1); SBAR();
    SBAR();
    bcc[5] = MFMA(wf9, hs[13], bcc[5]);
    SBAR();
    wf11 = ldb128(rb3 + 16384);
    SBAR();
    ui1 = FMA(-pfg[1], ua, -zi1); PIN(ui1); SBAR(); ur1 = FMA(-pfg[1], ub, zr1); PIN(ur1); SBAR();
    SBAR();
    ui1 = FMA(-pfg[0], ub, ui1); PIN(ui1); SBAR(); ur1 = FMA(pfg[0], ua, ur1); PIN(ur1); SBAR(); oi0 = cvtpk(ui0, ui1); PIN(oi0); SBAR(); or0 = cvtpk(ur0, ur1); PIN(or0); SBAR();
    SBAR();
    GL(gci, ROT9(2) * 73728 + 256, voffR, 114688);
    SBAR();
    bcc[5] = MFMA(wf10, hs[14], bcc[5]);
    SBAR();
    wf0 = ldb128(rb0 + 24576);
    wf1 = ldb128(rb1 + 24576);
    SBAR();
    t2_ = pfl[1] * zi1; PIN(t2_); SBAR(); t3_ = pfl[1] * zr1; PIN(t3_); SBAR(); ua = bflo(hlr[5]); PIN(ua); SBAR(); ub = bflo(hli[5]); PIN(ub); SBAR();
    SBAR();
    zi0 = FMA(pfl[0], zi1, t3_); PIN(zi0); SBAR(); zr0 = FMA(pfl[0], zr1, -t2_); PIN(zr0); SBAR();
    SBAR();
    bcc[5] = MFMA(wf11, hs[15], bcc[5]);
    SBAR();
    ui0 = FMA(-pfg[1], ua, -zi0); PIN(ui0); SBAR(); ur0 = FMA(-pfg[1], ub, zr0); PIN(ur0); SBAR();
    SBAR();
    ui0 = FMA(-pfg[0], ub, ui0); PIN(ui0); SBAR(); ur0 = FMA(pfg[0], ua, ur0); PIN(ur0); SBAR();
    SBAR();
    asm volatile("s_waitcnt vmcnt(6)\n\ts_barrier" ::: "memory"); SBAR();
    bcc[6] = MFMA(wf0, hs[8], bcc[6]);
    SBAR();
    wf2 = ldb128(rb2 + 24576);
    SBAR();
    t2_ = pfl[1] * zi0; PIN(t2_); SBAR(); t3_ = pfl[1] * zr0; PIN(t3_); SBAR(); ua = bfhi(hlr[5]); PIN(ua); SBAR(); ub = bfhi(hli[5]); PIN(ub); SBAR();
    SBAR();
    zi1 = FMA(pfl[0], zi0, t3_); PIN(zi1); SBAR(); zr1 = FMA(pfl[0], zr0, -t2_); PIN(zr1); SBAR();
    SBAR();
    bcc[6] = MFMA(wf1, hs[9], bcc[6]);
    SBAR();
    wf3 = ldb128(rb3 + 24576);
    SBAR();
    ui1 = FMA(-pfg[1], ua, -zi1); PIN(ui1); SBAR(); ur1 = FMA(-pfg[1], ub, zr1); PIN(ur1); SBAR();
    SBAR();
    ui1 = FMA(-pfg[0], ub, ui1); PIN(ui1); SBAR(); ur1 = FMA(pfg[0], ua, ur1); PIN(ur1); SBAR(); oi1 = cvtpk(ui0, ui1); PIN(oi1); SBAR(); or1 = cvtpk(ur0, ur1); PIN(or1); SBAR();
    SBAR();
    GL(gcr, ROT9(3) * 73728 + 256, voffR, 0);
    SBAR();
    bcc[6] = MFMA(wf2, hs[10], bcc[6]);
    SBAR();
    wf4 = ldb128(rb0 + 32768);
    SBAR();
    t2_ = pfl[1] * zi1; PIN(t2_); SBAR(); t3_ = pfl[1] * zr1; PIN(t3_); SBAR(); ua = bflo(hlr[6]); PIN(ua); SBAR(); ub = bflo(hli[6]); PIN(ub); SBAR();
    SBAR();
    zi0 = FMA(pfl[0], zi1, t3_); PIN(zi0); SBAR(); zr0 = FMA(pfl[0], zr1, -t2_); PIN(zr0); SBAR();
    SBAR();
    bcc[6] = MFMA(wf3, hs[11], bcc[6]);
    SBAR();
    wf5 = ldb128(rb1 + 32768);
    SBAR();
    ui0 = FMA(-pfg[1], ua, -zi0); PIN(ui0); SBAR(); ur0 = FMA(-pfg[1], ub, zr0); PIN(ur0); SBAR();
    SBAR();
    ui0 = FMA(-pfg[0], ub, ui0); PIN(ui0); SBAR(); ur0 = FMA(pfg[0], ua, ur0); PIN(ur0); SBAR();
    SBAR();
    bcc[6] = MFMA(wf4, hs[12], bcc[6]);
    SBAR();
    wf6 = ldb128(rb2 + 32768);
    SBAR();
    t2_ = pfl[1] * zi0; PIN(t2_); SBAR(); t3_ = pfl[1] * zr0; PIN(t3_); SBAR(); ua = bfhi(hlr[6]); PIN(ua); SBAR(); ub = bfhi(hli[6]); PIN(ub); SBAR();
    SBAR();
    zi1 = FMA(pfl[0], zi0, t3_); PIN(zi1); SBAR(); zr1 = FMA(pfl[0], zr0, -t2_); PIN(zr1); SBAR();
    SBAR();
    bcc[6] = MFMA(wf5, hs[13], bcc[6]);
    SBAR();
    wf7 = ldb128(rb3 + 32768);
    SBAR();
    ui1 = FMA(-pfg[1], ua, -zi1); PIN(ui1); SBAR(); ur1 = FMA(-pfg[1], ub, zr1); PIN(ur1); SBAR();
    SBAR();
    ui1 = FMA(-pfg[0], ub, ui1); PIN(ui1); SBAR(); ur1 = FMA(pfg[0], ua, ur1); PIN(ur1); SBAR(); oi2 = cvtpk(ui0, ui1); PIN(oi2); SBAR(); or2 = cvtpk(ur0, ur1); PIN(or2); SBAR();
    SBAR();
    GL(gci, ROT9(3) * 73728 + 256, voffR, 8192);
    SBAR();
    bcc[6] = MFMA(wf6, hs[14], bcc[6]);
    SBAR();
    wf8 = ldb128(rb0 + 40960);
    SBAR();
    t2_ = pfl[1] * zi1; PIN(t2_); SBAR(); t3_ = pfl[1] * zr1; PIN(t3_); SBAR(); ua = bflo(hlr[7]); PIN(ua); SBAR(); ub = bflo(hli[7]); PIN(ub); SBAR();
    SBAR();
    zi0 = FMA(pfl[0], zi1, t3_); PIN(zi0); SBAR(); zr0 = FMA(pfl[0], zr1, -t2_); PIN(zr0); SBAR();
    SBAR();
    bcc[6] = MFMA(wf7, hs[15], bcc[6]);
    SBAR();
    wf9 = ldb128(rb1 + 40960);
    SBAR();
    ui0 = FMA(-pfg[1], ua, -zi0); PIN(ui0); SBAR(); ur0 = FMA(-pfg[1], ub, zr0); PIN(ur0); SBAR();
    SBAR();
    ui0 = FMA(-pfg[0], ub, ui0); PIN(ui0); SBAR(); ur0 = FMA(pfg[0], ua, ur0); PIN(ur0); SBAR();
    SBAR();
    bcc[7] = MFMA(wf8, hs[8], bcc[7]);
    SBAR();
    wf10 = ldb128(rb2 + 40960);
    SBAR();
    t2_ = pfl[1] * zi0; PIN(t2_); SBAR(); t3_ = pfl[1] * zr0; PIN(t3_); SBAR(); ua = bfhi(hlr[7]); PIN(ua); SBAR(); ub = bfhi(hli[7]); PIN(ub); SBAR();
    SBAR();
    zi1 = FMA(pfl[0], zi0, t3_); PIN(zi1); SBAR(); zr1 = FMA(pfl[0], zr0, -t2_); PIN(zr1); SBAR();
    SBAR();
    bcc[7] = MFMA(wf9, hs[9], bcc[7]);
    SBAR();
    wf11 = ldb128(rb3 + 40960);
    SBAR();
    ui1 = FMA(-pfg[1], ua, -zi1); PIN(ui1); SBAR(); ur1 = FMA(-pfg[1], ub, zr1); PIN(ur1); SBAR();
    SBAR();
    ui1 = FMA(-pfg[0], ub, ui1); PIN(ui1); SBAR(); ur1 = FMA(pfg[0], ua, ur1); PIN(ur1); SBAR(); oi3 = cvtpk(ui0, ui1); PIN(oi3); SBAR(); or3 = cvtpk(ur0, ur1); PIN(or3); SBAR(); { const lptr p_ = HW(1); stu4(p_ + 4096, (u32x4){oi0, oi1, oi2, oi3}); stu4(p_, (u32x4){or0, or1, or2, or3}); }
    SBAR();
    GL(gcr, ROT9(4) * 73728 + 256, voffR, 16384);
    SBAR();
    bcc[7] = MFMA(wf10, hs[10], bcc[7]);
    SBAR();
    rb0 += 49152; PIN(rb0); wf0 = ldb128(rb0 + 0);
    rb1 += 49152; PIN(rb1); wf1 = ldb128(rb1 + 0);
    SBAR();
    bcc[7] = MFMA(wf11, hs[11], bcc[7]);
    SBAR();
    asm volatile("s_waitcnt vmcnt(6) lgkmcnt(2)\n\ts_barrier" ::: "memory"); SBAR();
    bcc[7] = MFMA(wf0, hs[12], bcc[7]);
    SBAR();
    rb2 += 49152; PIN(rb2); wf2 = ldb128(rb2 + 0);
    SBAR();
    bcc[7] = MFMA(wf1, hs[13], bcc[7]);
    SBAR();
    rb3 += 49152; PIN(rb3); wf3 = ldb128(rb3 + 0);
    SBAR();
    hs[0] = trfrag2(ht0 + 0, ht1 + 0);
    SBAR();
    GL(gci, ROT9(4) * 73728 + 256, voffR, 24576);
    SBAR();
    bcc[7] = MFMA(wf2, hs[14], bcc[7]);
    SBAR();
    wf4 = ldb128(rb0 + 8192);
    SBAR();
    hs[1] = trfrag2(ht0 + 1024, ht1 + 1024);
    SBAR();
    bcc[7] = MFMA(wf3, hs[15], bcc[7]);
    SBAR();
    wf5 = ldb128(rb1 + 8192);
    SBAR();
    hs[2] = trfrag2(ht0 + 2048, ht1 + 2048);
    SBAR();
    bcc[8] = MFMA(wf4, hs[8], bcc[8]);
    SBAR();
    wf6 = ldb128(rb2 + 8192);
    SBAR();
    hs[3] = trfrag2(ht0 + 3072, ht1 + 3072);
    SBAR();
    bcc[8] = MFMA(wf5, hs[9], bcc[8]);
    SBAR();
    wf7 = ldb128(rb3 + 8192);
    SBAR();
    hs[4] = trfrag2(ht0 + 4096, ht1 + 4096);
    SBAR();
    GL(gcr, ROT9(5) * 73728 + 256, voffR, 32768);
    SBAR();
    bcc[8] = MFMA(wf6, hs[10], bcc[8]);
    SBAR();
    wf8 = ldb128(rb0 + 16384);
    SBAR();
    hs[5] = trfrag2(ht0 + 5120, ht1 + 5120);
    SBAR();
    bcc[8] = MFMA(wf7, hs[11], bcc[8]);
    SBAR();
    wf9 = ldb128(rb1 + 16384);
    SBAR();
    hs[6] = trfrag2(ht0 + 6144, ht1 + 6144);
    SBAR();
    bcc[8] = MFMA(wf8, hs[12], bcc[8]);
    SBAR();
    wf10 = ldb128(rb2 + 16384);
    SBAR();
    hs[7] = trfrag2(ht0 + 7168, ht1 + 7168);
    SBAR();
    bcc[8] = MFMA(wf9, hs[13], bcc[8]);
    SBAR();
    wf11 = ldb128(rb3 + 16384);
    SBAR();
    GL(gci, ROT9(5) * 73728 + 256, voffR, 40960);
    SBAR();
    bcc[8] = MFMA(wf10, hs[14], bcc[8]);
    SBAR();
    wf0 = ldb128(rb0 + 24576);
    wf1 = ldb128(rb1 + 24576);
    SBAR();
    bcc[8] = MFMA(wf11, hs[15], bcc[8]);
    SBAR();
    asm volatile("s_waitcnt vmcnt(6)\n\ts_barrier" ::: "memory"); SBAR();
    DEF_PRS();
    bcc[0] = MFMA(wf0, hs[0], bcc[0]);
    SBAR();
    wf2 = ldb128(rb2 + 24576);
    SBAR();
    bcc[0] = MFMA(wf1, hs[1], bcc[0]);
    SBAR();
    wf3 = ldb128(rb3 + 24576);
    SBAR();
    GL(gcr, ROT9(6) * 73728 + 256, voffR, 49152);
    SBAR();
    bcc[0] = MFMA(wf2, hs[2], bcc[0]);
    SBAR();
    wf4 = ldb128(rb0 + 32768);
    SBAR();
    bcc[0] = MFMA(wf3, hs[3], bcc[0]);
    SBAR();
    wf5 = ldb128(rb1 + 32768);
    SBAR();
    bcc[0] = MFMA(wf4, hs[4], bcc[0]);
    SBAR();
    wf6 = ldb128(rb2 + 32768);
    SBAR();
    bcc[0] = MFMA(wf5, hs[5], bcc[0]);
    SBAR();
    wf7 = ldb128(rb3 + 32768);
    SBAR();
    GL(gci, ROT9(6) * 73728 + 256, voffR, 57344);
    SBAR();
    bcc[0] = MFMA(wf6, hs[6], bcc[0]);
    SBAR();
    wf8 = ldb128(rb0 + 40960);
    SBAR();
    bcc[0] = MFMA(wf7, hs[7], bcc[0]);
    SBAR();
    wf9 = ldb128(rb1 + 40960);
    SBAR();
    bcc[1] = MFMA(wf8, hs[0], bcc[1]);
    SBAR();
    wf10 = ldb128(rb2 + 40960);
    SBAR();
    bcc[1] = MFMA(wf9, hs[1], bcc[1]);
    SBAR();
    wf11 = ldb128(rb3 + 40960);
    SBAR();
    pk0[0] = cvtpk(bcc[0][0], bcc[0][1]);
    SBAR();
    pk0[1] = cvtpk(bcc[0][2], bcc[0][3]);
    SBAR();
    GL(gcr, ROT9(7) * 73728 + 256, voffR, 65536);
    SBAR();
    bcc[1] = MFMA(wf10, hs[2], bcc[1]);
    SBAR();
    rb0 += 49152; PIN(rb0); wf0 = ldb128(rb0 + 0);
    rb1 += 49152; PIN(rb1); wf1 = ldb128(rb1 + 0);
    SBAR();
    pk0[2] = cvtpk(bcc[0][4], bcc[0][5]);
    SBAR();
    pk0[3] = cvtpk(bcc[0][6], bcc[0][7]); PIN(pk0);
    SBAR();
    bcc[1] = MFMA(wf11, hs[3], bcc[1]);
    SBAR();
    stpart(pk0, ROT9(0) * 114688);
    SBAR();
    asm volatile("s_waitcnt vmcnt(7)\n\ts_barrier" ::: "memory"); SBAR();
    bcc[1] = MFMA(wf0, hs[4], bcc[1]);
    SBAR();
    rb2 += 49152; PIN(rb2); wf2 = ldb128(rb2 + 0);
    SBAR();
    pk1[0] = cvtpk(bcc[0][8], bcc[0][9]);
    SBAR();
    pk1[1] = cvtpk(bcc[0][10], bcc[0][11]);
    SBAR();
    bcc[1] = MFMA(wf1, hs[5], bcc[1]);
    SBAR();
    rb3 += 49152; PIN(rb3); wf3 = ldb128(rb3 + 0);
    SBAR();
    pk1[2] = cvtpk(bcc[0][12], bcc[0][13]);
    SBAR();
    pk1[3] = cvtpk(bcc[0][14], bcc[0][15]); PIN(pk1);
    SBAR();
    GL(gci, ROT9(7) * 73728 + 256, voffR, 73728);
    SBAR();
    bcc[1] = MFMA(wf2, hs[6], bcc[1]);
    SBAR();
    wf4 = ldb128(rb0 + 8192);
    SBAR();
    stpart(pk1, ROT9(0) * 114688 + 1024);
    SBAR();
    bcc[1] = MFMA(wf3, hs[7], bcc[1]);
    SBAR();
    wf5 = ldb128(rb1 + 8192);
    SBAR();
    bcc[2] = MFMA(wf4, hs[0], bcc[2]);
    SBAR();
    wf6 = ldb128(rb2 + 8192);
    SBAR();
    bcc[2] = MFMA(wf5, hs[1], bcc[2]);
    SBAR();
    wf7 = ldb128(rb3 + 8192);
    SBAR();
    pk0[0] = cvtpk(bcc[1][0], bcc[1][1]);
    SBAR();
    pk0[1] = cvtpk(bcc[1][2], bcc[1][3]);
    SBAR();
    GL(gcr, ROT9(8) * 73728 + 256, voffR, 81920);
    SBAR();
    bcc[2] = MFMA(wf6, hs[2], bcc[2]);
    SBAR();
    wf8 = ldb128(rb0 + 16384);
    SBAR();
    pk0[2] = cvtpk(bcc[1][4], bcc[1][5]);
    SBAR();
    pk0[3] = cvtpk(bcc[1][6], bcc[1][7]); PIN(pk0);
    SBAR();
    bcc[2] = MFMA(wf7, hs[3], bcc[2]);
    SBAR();
    wf9 = ldb128(rb1 + 16384);
    SBAR();
    stpart(pk0, ROT9(1) * 114688);
    SBAR();
    bcc[2] = MFMA(wf8, hs[4], bcc[2]);
    SBAR();
    wf10 = ldb128(rb2 + 16384);
    SBAR();
    pk1[0] = cvtpk(bcc[1][8], bcc[1][9]);
    SBAR();
    pk1[1] = cvtpk(bcc[1][10], bcc[1][11]);
    SBAR();
    bcc[2] = MFMA(wf9, hs[5], bcc[2]);
    SBAR();
    wf11 = ldb128(rb3 + 16384);
    SBAR();
    pk1[2] = cvtpk(bcc[1][12], bcc[1][13]);
    SBAR();
    pk1[3] = cvtpk(bcc[1][14], bcc[1][15]); PIN(pk1);
    SBAR();
    GL(gci, ROT9(8) * 73728 + 256, voffR, 90112);
    SBAR();
    bcc[2] = MFMA(wf10, hs[6], bcc[2]);
    SBAR();
    rb0 += -98304; PIN(rb0); wf0 = ldb128(rb0 + 0);
    rb1 += -98304; PIN(rb1); wf1 = ldb128(rb1 + 0);
    SBAR();
    stpart(pk1, ROT9(1) * 114688 + 1024);
    SBAR();
    bcc[2] = MFMA(wf11, hs[7], bcc[2]);
    SBAR();
    asm volatile("s_waitcnt vmcnt(10)\n\ts_barrier" ::: "memory"); SBAR();
    bcc[3] = MFMA(wf0, hs[0], bcc[3]);
    SBAR();
    rb2 += -98304; PIN(rb2); wf2 = ldb128(rb2 + 0);
    SBAR();
    pk0[0] = cvtpk(bcc[2][0], bcc[2][1]);
    SBAR();
    pk0[1] = cvtpk(bcc[2][2], bcc[2][3]);
    SBAR();
    bcc[3] = MFMA(wf1, hs[1], bcc[3]);
    SBAR();
    rb3 += -98304; PIN(rb3); wf3 = ldb128(rb3 + 0);
    SBAR();
    pk0[2] = cvtpk(bcc[2][4], bcc[2][5]);
    SBAR();
    pk0[3] = cvtpk(bcc[2][6], bcc[2][7]); PIN(pk0);
    SBAR();
    bcc[3] = MFMA(wf2, hs[2], bcc[3]);
    SBAR();
    wf4 = ldb128(rb0 + 8192);
    SBAR();
    stpart(pk0, ROT9(2) * 114688);
    SBAR();
    bcc[3] = MFMA(wf3, hs[3], bcc[3]);
    SBAR();
    wf5 = ldb128(rb1 + 8192);
    SBAR();
    pk1[0] = cvtpk(bcc[2][8], bcc[2][9]);
    SBAR();
    pk1[1] = cvtpk(bcc[2][10], bcc[2][11]);
    SBAR();
    bcc[3] = MFMA(wf4, hs[4], bcc[3]);
    SBAR();
    wf6 = ldb128(rb2 + 8192);
    SBAR();
    pk1[2] = cvtpk(bcc[2][12], bcc[2][13]);
    SBAR();
    pk1[3] = cvtpk(bcc[2][14], bcc[2][15]); PIN(pk1);
    SBAR();
    bcc[3] = MFMA(wf5, hs[5], bcc[3]);
    SBAR();
    wf7 = ldb128(rb3 + 8192);
    SBAR();
    stpart(pk1, ROT9(2) * 114688 + 1024);
    SBAR();
    bcc[3] = MFMA(wf6, hs[6], bcc[3]);
    SBAR();
    wf8 = ldb128(rb0 + 16384);
    SBAR();
    bcc[3] = MFMA(wf7, hs[7], bcc[3]);
    SBAR();
    wf9 = ldb128(rb1 + 16384);
    SBAR();
    bcc[4] = MFMA(wf8, hs[0], bcc[4]);
    SBAR();
    wf10 = ldb128(rb2 + 16384);
    SBAR();
    bcc[4] = MFMA(wf9, hs[1], bcc[4]);
    SBAR();
    wf11 = ldb128(rb3 + 16384);
    SBAR();
    pk0[0] = cvtpk(bcc[3][0], bcc[3][1]);
    SBAR();
    pk0[1] = cvtpk(bcc[3][2], bcc[3][3]);
    SBAR();
    bcc[4] = MFMA(wf10, hs[2], bcc[4]);
    SBAR();
    wf0 = ldb128(rb0 + 24576);
    wf1 = ldb128(rb1 + 24576);
    SBAR();
    pk0[2] = cvtpk(bcc[3][4], bcc[3][5]);
    SBAR();
    pk0[3] = cvtpk(bcc[3][6], bcc[3][7]); PIN(pk0);
    SBAR();
    bcc[4] = MFMA(wf11, hs[3], bcc[4]);
    SBAR();
    stpart(pk0, ROT9(3) * 114688);
    SBAR();
    asm volatile("s_waitcnt vmcnt(10)\n\ts_barrier" ::: "memory"); SBAR();
    bcc[4] = MFMA(wf0, hs[4], bcc[4]);
    SBAR();
    wf2 = ldb128(rb2 + 24576);
    SBAR();
    pk1[0] = cvtpk(bcc[3][8], bcc[3][9]);
    SBAR();
    pk1[1] = cvtpk(bcc[3][10], bcc[3][11]);
    SBAR();
    bcc[4] = MFMA(wf1, hs[5], bcc[4]);
    SBAR();
    wf3 = ldb128(rb3 + 24576);
    SBAR();
    pk1[2] = cvtpk(bcc[3][12], bcc[3][13]);
    SBAR();
    pk1[3] = cvtpk(bcc[3][14], bcc[3][15]); PIN(pk1);
    SBAR();
    bcc[4] = MFMA(wf2, hs[6], bcc[4]);
    SBAR();
    wf4 = ldb128(rb0 + 32768);
    SBAR();
    stpart(pk1, ROT9(3) * 114688 + 1024);
    SBAR();
    bcc[4] = MFMA(wf3, hs[7], bcc[4]);
    SBAR();
    wf5 = ldb128(rb1 + 32768);
    SBAR();
    bcc[5] = MFMA(wf4, hs[0], bcc[5]);
    SBAR();
    wf6 = ldb128(rb2 + 32768);
    SBAR();
    bcc[5] = MFMA(wf5, hs[1], bcc[5]);
    SBAR();
    wf7 = ldb128(rb3 + 32768);
    SBAR();
    pk0[0] = cvtpk(bcc[4][0], bcc[4][1]);
    SBAR();
    pk0[1] = cvtpk(bcc[4][2], bcc[4][3]);
    SBAR();
    bcc[5] = MFMA(wf6, hs[2], bcc[5]);
    SBAR();
    wf8 = ldb128(rb0 + 40960);
    SBAR();
    pk0[2] = cvtpk(bcc[4][4], bcc[4][5]);
    SBAR();
    pk0[3] = cvtpk(bcc[4][6], bcc[4][7]); PIN(pk0);
    SBAR();
    bcc[5] = MFMA(wf7, hs[3], bcc[5]);
    SBAR();
    wf9 = ldb128(rb1 + 40960);
    SBAR();
    stpart(pk0, ROT9(4) * 114688);
    SBAR();
    bcc[5] = MFMA(wf8, hs[4], bcc[5]);
    SBAR();
    wf10 = ldb128(rb2 + 40960);
    SBAR();
    pk1[0] = cvtpk(bcc[4][8], bcc[4][9]);
    SBAR();
    pk1[1] = cvtpk(bcc[4][10], bcc[4][11]);
    SBAR();
    bcc[5] = MFMA(wf9, hs[5], bcc[5]);
    SBAR();
    wf11 = ldb128(rb3 + 40960);
    SBAR();
    pk1[2] = cvtpk(bcc[4][12], bcc[4][13]);
    SBAR();
    pk1[3] = cvtpk(bcc[4][14], bcc[4][15]); PIN(pk1);
    SBAR();
    bcc[5] = MFMA(wf10, hs[6], bcc[5]);
    SBAR();
    rb0 += 49152; PIN(rb0); wf0 = ldb128(rb0 + 0);
    rb1 += 49152; PIN(rb1); wf1 = ldb128(rb1 + 0);
    SBAR();
    stpart(pk1, ROT9(4) * 114688 + 1024);
    SBAR();
    bcc[5] = MFMA(wf11, hs[7], bcc[5]);
    SBAR();
    asm volatile("s_waitcnt vmcnt(6)\n\ts_barrier" ::: "memory"); SBAR();
    bcc[6] = MFMA(wf0, hs[0], bcc[6]);
    SBAR();
    rb2 += 49152; PIN(rb2); wf2 = ldb128(rb2 + 0);
    SBAR();
    PROG(2u);
    SBAR();
    pk0[0] = cvtpk(bcc[5][0], bcc[5][1]);
    SBAR();
    pk0[1] = cvtpk(bcc[5][2], bcc[5][3]);
    SBAR();
    bcc[6] = MFMA(wf1, hs[1], bcc[6]);
    SBAR();
    rb3 += 49152; PIN(rb3); wf3 = ldb128(rb3 + 0);
    SBAR();
    pk0[2] = cvtpk(bcc[5][4], bcc[5][5]);
    SBAR();
    pk0[3] = cvtpk(bcc[5][6], bcc[5][7]); PIN(pk0);
    SBAR();
    bcc[6] = MFMA(wf2, hs[2], bcc[6]);
    SBAR();
    wf4 = ldb128(rb0 + 8192);
    SBAR();
    stpart(pk0, ROT9(5) * 114688);
    SBAR();
    bcc[6] = MFMA(wf3, hs[3], bcc[6]);
    SBAR();
    wf5 = ldb128(rb1 + 8192);
    SBAR();
    pk1[0] = cvtpk(bcc[5][8], bcc[5][9]);
    SBAR();
    pk1[1] = cvtpk(bcc[5][10], bcc[5][11]);
    SBAR();
    bcc[6] = MFMA(wf4, hs[4], bcc[6]);
    SBAR();
    wf6 = ldb128(rb2 + 8192);
    SBAR();
    pk1[2] = cvtpk(bcc[5][12], bcc[5][13]);
    SBAR();
    pk1[3] = cvtpk(bcc[5][14], bcc[5][15]); PIN(pk1);
    SBAR();
    bcc[6] = MFMA(wf5, hs[5], bcc[6]);
    SBAR();
    wf7 = ldb128(rb3 + 8192);
    SBAR();
    stpart(pk1, ROT9(5) * 114688 + 1024);
    SBAR();
    bcc[6] = MFMA(wf6, hs[6], bcc[6]);
    SBAR();
    wf8 = ldb128(rb0 + 16384);
    SBAR();
    bcc[6] = MFMA(wf7, hs[7], bcc[6]);
    SBAR();
    wf9 = ldb128(rb1 + 16384);
    SBAR();
    bcc[7] = MFMA(wf8, hs[0], bcc[7]);
    SBAR();
    wf10 = ldb128(rb2 + 16384);
    SBAR();
    bcc[7] = MFMA(wf9, hs[1], bcc[7]);
    SBAR();
    wf11 = ldb128(rb3 + 16384);
    SBAR();
    pk0[0] = cvtpk(bcc[6][0], bcc[6][1]);
    SBAR();
    pk0[1] = cvtpk(bcc[6][2], bcc[6][3]);
    SBAR();
    bcc[7] = MFMA(wf10, hs[2], bcc[7]);
    SBAR();
    wf0 = ldb128(rb0 + 24576);
    wf1 = ldb128(rb1 + 24576);
    SBAR();
    pk0[2] = cvtpk(bcc[6][4], bcc[6][5]);
    SBAR();
    pk0[3] = cvtpk(bcc[6][6], bcc[6][7]); PIN(pk0);
    SBAR();
    bcc[7] = MFMA(wf11, hs[3], bcc[7]);
    SBAR();
    stpart(pk0, ROT9(6) * 114688);
    SBAR();
    asm volatile("s_waitcnt vmcnt(7)\n\ts_barrier" ::: "memory"); SBAR();
    bcc[7] = MFMA(wf0, hs[4], bcc[7]);
    SBAR();
    wf2 = ldb128(rb2 + 24576);
    SBAR();
    PROG(3u);
    SBAR();
    pk1[0] = cvtpk(bcc[6][8], bcc[6][9]);
    SBAR();
    pk1[1] = cvtpk(bcc[6][10], bcc[6][11]);
    SBAR();
    bcc[7] = MFMA(wf1, hs[5], bcc[7]);
    SBAR();
    wf3 = ldb128(rb3 + 24576);
    SBAR();
    pk1[2] = cvtpk(bcc[6][12], bcc[6][13]);
    SBAR();
    pk1[3] = cvtpk(bcc[6][14], bcc[6][15]); PIN(pk1);
    SBAR();
    bcc[7] = MFMA(wf2, hs[6], bcc[7]);
    SBAR();
    wf4 = ldb128(rb0 + 32768);
    SBAR();
    stpart(pk1, ROT9(6) * 114688 + 1024);
    SBAR();
    bcc[7] = MFMA(wf3, hs[7], bcc[7]);
    SBAR();
    wf5 = ldb128(rb1 + 32768);
    SBAR();
    bcc[8] = MFMA(wf4, hs[0], bcc[8]);
    SBAR();
    wf6 = ldb128(rb2 + 32768);
    SBAR();
    bcc[8] = MFMA(wf5, hs[1], bcc[8]);
    SBAR();
    wf7 = ldb128(rb3 + 32768);
    SBAR();
    pk0[0] = cvtpk(bcc[7][0], bcc[7][1]);
    SBAR();
    pk0[1] = cvtpk(bcc[7][2], bcc[7][3]);
    SBAR();
    bcc[8] = MFMA(wf6, hs[2], bcc[8]);
    SBAR();
    wf8 = ldb128(rb0 + 40960);
    SBAR();
    pk0[2] = cvtpk(bcc[7][4], bcc[7][5]);
    SBAR();
    pk0[3] = cvtpk(bcc[7][6], bcc[7][7]); PIN(pk0);
    SBAR();
    bcc[8] = MFMA(wf7, hs[3], bcc[8]);
    SBAR();
    wf9 = ldb128(rb1 + 40960);
    SBAR();
    stpart(pk0, ROT9(7) * 114688);
    SBAR();
    bcc[8] = MFMA(wf8, hs[4], bcc[8]);
    SBAR();
    wf10 = ldb128(rb2 + 40960);
    SBAR();
    pk1[0] = cvtpk(bcc[7][8], bcc[7][9]);
    SBAR();
    pk1[1] = cvtpk(bcc[7][10], bcc[7][11]);
    SBAR();
    bcc[8] = MFMA(wf9, hs[5], bcc[8]);
    SBAR();
    wf11 = ldb128(rb3 + 40960);
    SBAR();
    pk1[2] = cvtpk(bcc[7][12], bcc[7][13]);
    SBAR();
    pk1[3] = cvtpk(bcc[7][14], bcc[7][15]); PIN(pk1);
    SBAR();
    bcc[8] = MFMA(wf10, hs[6], bcc[8]);
    SBAR();
    stpart(pk1, ROT9(7) * 114688 + 1024);
    SBAR();
    bcc[8] = MFMA(wf11, hs[7], bcc[8]);
    SBAR();
    pk0[0] = cvtpk(bcc[8][0], bcc[8][1]);
    pk0[1] = cvtpk(bcc[8][2], bcc[8][3]);
    pk0[2] = cvtpk(bcc[8][4], bcc[8][5]);
    pk0[3] = cvtpk(bcc[8][6], bcc[8][7]); PIN(pk0);
    stpart(pk0, ROT9(8) * 114688);
    pk1[0] = cvtpk(bcc[8][8], bcc[8][9]);
    pk1[1] = cvtpk(bcc[8][10], bcc[8][11]);
    pk1[2] = cvtpk(bcc[8][12], bcc[8][13]);
    pk1[3] = cvtpk(bcc[8][14], bcc[8][15]); PIN(pk1);
    stpart(pk1, ROT9(8) * 114688 + 1024);
    }
    f_rt = rt; f_me = slice; f_tag = tag;
    }
    LANE2(lane);
    u64* dn = (u64*)(ws + WS_DONE) + f_rt * 32;
    if (f_me >= 12) {
        asm volatile("s_waitcnt vmcnt(0)\n\ts_barrier" ::: "memory");
        if (wave == 0 && lane == 0 && f_me < 15) __hip_atomic_store(dn + f_me, ((u64)f_tag << 32) | 9u, __ATOMIC_RELAXED, __HIP_MEMORY_SCOPE_AGENT);
    } else { const int g4 = f_me / 3, m3 = f_me - 3 * g4, k = lane & 7, src = k < 4 ? m3 + 3 * k : k < 7 ? 8 + k : f_me;
        volatile LAS unsigned* pg = (volatile LAS unsigned*)(L + LDS_BYTES - 256);
        if (wave == 7) pg[lane] = src == f_me ? 9u : 0u;
        asm volatile("s_waitcnt vmcnt(0) lgkmcnt(0)\n\ts_barrier" ::: "memory");
        if (wave == 7) {
            if (lane == 0) __hip_atomic_store(dn + f_me, ((u64)f_tag << 32) | 9u, __ATOMIC_RELAXED, __HIP_MEMORY_SCOPE_AGENT);
            for (unsigned s = 0; s < SPIN_MAX; ++s) { const u64 g = __hip_atomic_load(dn + src, __ATOMIC_RELAXED, __HIP_MEMORY_SCOPE_AGENT);
                const unsigned p = src == f_me ? 9u : (unsigned)(g >> 32) == f_tag ? (unsigned)g : 0u; pg[lane] = p;
                if (__all(p >= 9u)) break; __builtin_amdgcn_s_sleep(2); }
            pg[lane] = 9u;
        } else if (wave < 6) { Red r, r2; const int ct = f_ct;
            { const lptr pd = L + 4 * SLOT + 45056 + (ct - 6 * m3) * 64 + 8 * (lane >> 5); const lptr px = L + 4 * SLOT + 32768 + ((ct - 6 * m3) >> 1) * 4096 + (lane & 31) * 128 + (ct & 1) * 64 + 8 * (lane >> 5);
#pragma unroll
              for (int i = 0; i < 2; ++i) { r.dv[i] = *(const LAS u32x2*)(pd + 16 * i); r2.dv[i] = *(const LAS u32x2*)(pd + 32 + 16 * i); r.xv[i] = *(const LAS u32x2*)(px + 16 * i); r2.xv[i] = *(const LAS u32x2*)(px + 32 + 16 * i); } }
            r.v[0] = *(const LAS u32x4*)(L + AUX + 512 + wave * 2048 + lane * 16); r2.v[0] = *(const LAS u32x4*)(L + AUX + 1536 + wave * 2048 + lane * 16);
            r.st = r2.st = *(const LAS unsigned*)(L + AUX + (lane & 31) * 4);
            const bf16x8 sel0 = red_sel(lane, 0), sel1 = red_sel(lane, 1);
            const int j9 = (ct >> 1) + 9 - f_rt % 9; const unsigned need = k < 4 ? 7 + f_jj : k < 7 ? 1 + (j9 >= 9 ? j9 - 9 : j9) : 0;
            for (unsigned s = 0; s < SPIN_MAX * 16u; ++s) { if (__all((k >= 4 && k < 7) || pg[lane] >= need)) break; __builtin_amdgcn_s_sleep(1); }
            asm volatile("" ::: "memory");
            red_load<1, 4>(r, f_rt, ct, g4, 0, lane, ws); red_load<1, 4>(r2, f_rt, ct, g4, 1, lane, ws);
            const bool early = __all(pg[lane] >= need);
            asm volatile("" ::: "memory");
            if (early) { red_load<4, 7>(r, f_rt, ct, g4, 0, lane, ws); red_load<4, 7>(r2, f_rt, ct, g4, 1, lane, ws); }
            red_sum<0, 4>(r, sel0); red_sum<0, 4>(r2, sel0); red_gate(r); red_gate(r2);
            if (!early) {
                for (unsigned s = 0; s < SPIN_MAX * 16u; ++s) { if (__all(pg[lane] >= need)) break; __builtin_amdgcn_s_sleep(1); }
                asm volatile("" ::: "memory");
                red_load<4, 7>(r, f_rt, ct, g4, 0, lane, ws); red_load<4, 7>(r2, f_rt, ct, g4, 1, lane, ws); }
            red_sum<4, 7>(r, sel1); red_sum<4, 7>(r2, sel1);
            PIN(r.s);
            const lptr img = L + wave * 4096; red_finish(r, 0, lane, img); red_finish(r2, 1, lane, img);
            float* po = out + (size_t)(f_rt * 128 + g4 * 32 + (lane >> 3)) * 576 + ct * 32 + (lane & 7) * 4;
#pragma unroll
            for (int i = 0; i < 4; ++i) { const f32x4 o = ldf4(img + (8 * i + (lane >> 3)) * 128 + (((lane & 7) ^ (lane >> 3)) << 4));
                __builtin_amdgcn_raw_buffer_store_b128(__builtin_bit_cast(u32x4, o), __builtin_amdgcn_make_buffer_rsrc(out, 0, 2048 * 576 * 4, 0x00020000), (unsigned)((po - out) + i * 8 * 576) * 4, 0, 16); }
        }
    }
    asm volatile("k1_end:");
}

extern "C" void kernel_launch(void* const* d_in, const int* in_sizes, int n_in, void* d_out, int out_size, void* d_ws, size_t ws_size, hipStream_t stream) {
    (void)hipFuncSetAttribute((const void*)k1, hipFuncAttributeMaxDynamicSharedMemorySize, LDS_BYTES);
    k1<<<256, 512, LDS_BYTES, stream>>>((const char*)d_in[0], (const char*)d_in[1], (const char*)d_in[2], (const char*)d_in[3], (const char*)d_in[4], (const char*)d_in[5], (const char*)d_in[6],
                                        (const char*)d_in[7], (const char*)d_in[10], (const char*)d_in[11], (const char*)d_in[12], (const char*)d_in[8], (char*)d_ws, (float*)d_out);
}
```
